```python
import torch

def _softmax(x, axis=-1):
    return torch.softmax(x, dim=axis)
def _gelu(x):
    return torch.nn.functional.gelu(x, approximate="tanh")
def _silu(x):
    return torch.nn.functional.silu(x)
def _sigmoid(x):
    return torch.sigmoid(x)
def _layernorm(x, axis=-1, eps=1e-5):
    m = x.mean(dim=axis, keepdim=True)
    v = x.var(dim=axis, keepdim=True, unbiased=False)
    return (x - m) / torch.sqrt(v + eps)
def _scatter_add(tgt, idx, src, axis=0):
    return tgt.clone().index_add_(axis, idx, src)
def _segment_sum(src, seg, n):
    out = torch.zeros((n,) + src.shape[1:], dtype=src.dtype)
    return out.index_add_(0, seg, src)
def _pad(x, pw):
    flat = tuple(v for pair in reversed(pw) for v in pair)
    return torch.nn.functional.pad(x, flat)

kernel_name = 'interleave_layers__sliding_local_dilated__glu_gated_reglu__prompt'

def setup_inputs():

    _w = (lambda a: (lambda t: t.to(torch.bfloat16) if t.is_floating_point() else t)(torch.as_tensor(a)))
    def _A_setup():
        B, S, D = 1, 2048, 576
        return {
            "x": _w((lambda *s: torch.randn(*s, dtype=torch.bfloat16) * 0.05)(B, S, D)),
            "wqkv": _w((lambda *s: torch.randn(*s, dtype=torch.bfloat16) * 0.05)(D, 3 * D)),
            "wo": _w((lambda *s: torch.randn(*s, dtype=torch.bfloat16) * 0.05)(D, D)),
        }
    _A_in = _A_setup()

    def _B_setup():
        B, S, D, F = 1, 2048, 576, 4608
        return {
            "x": _w((lambda *s: torch.randn(*s, dtype=torch.bfloat16) * 0.05)(B, S, D)),
            "W_gate": _w((lambda *s: torch.randn(*s, dtype=torch.bfloat16) * 0.8)(D, F) * (2.0 / D) ** 0.5),
            "W_up": _w((lambda *s: torch.randn(*s, dtype=torch.bfloat16) * 0.8)(D, F) * (2.0 / D) ** 0.5),
            "W_down": _w((lambda *s: torch.randn(*s, dtype=torch.bfloat16) * 0.8)(F, D) * (1.0 / F) ** 0.5),
        }
    _B_in = _B_setup()

    return {
        "A_x": _A_in["x"],
        "A_wqkv": _A_in["wqkv"],
        "A_wo": _A_in["wo"],
        "B_x": _B_in["x"],
        "B_W_gate": _B_in["W_gate"],
        "B_W_up": _B_in["W_up"],
        "B_W_down": _B_in["W_down"],
    }

def reference(A_x, A_wqkv, A_wo, B_x, B_W_gate, B_W_up, B_W_down):
    def _A_ref(x, wqkv, wo):
        B, S, D = x.shape
        H = 32; Dh = D // H
        W, dil = 8, 2
        qkv = (lambda x, *d: x.permute(*d))((x @ wqkv).reshape(B, S, 3, H, Dh), 2, 0, 3, 1, 4)
        q, k, v = qkv[0], qkv[1], qkv[2]
        scale = Dh ** -0.5
        pos = torch.arange(S)
        off = torch.arange(W) * dil
        idx = pos[:, None] - off[None, :]
        mask = idx >= 0
        idx_c = torch.where(mask, idx, 0).reshape(-1)
        kg = k[:, :, idx_c, :].reshape(B, H, S, W, Dh)
        vg = v[:, :, idx_c, :].reshape(B, H, S, W, Dh)
        logits = torch.einsum("bhsd,bhswd->bhsw", q, kg) * scale
        logits = torch.where(mask[None, None, :, :], logits, -1e9)
        attn = _softmax(logits, axis=-1)
        o = torch.einsum("bhsw,bhswd->bhsd", attn, vg)
        return (lambda x, *d: x.permute(*d))(o, 0, 2, 1, 3).reshape(B, S, D) @ wo

    def _B_ref(x, W_gate, W_up, W_down):
        gate = torch.einsum("bsd,df->bsf", x, W_gate)
        up = torch.einsum("bsd,df->bsf", x, W_up)
        gate = gate * (lambda x, dt: x.to(dt))(gate > 0, gate.dtype)
        h = gate * up
        return torch.einsum("bsf,fd->bsd", h, W_down)

    x = A_x
    x = x + _A_ref(_layernorm(x, axis=-1), A_wqkv, A_wo)
    x = x + _B_ref(_layernorm(x, axis=-1), B_W_gate, B_W_up, B_W_down)
    x = x + _A_ref(_layernorm(x, axis=-1), A_wqkv, A_wo)
    x = x + _B_ref(_layernorm(x, axis=-1), B_W_gate, B_W_up, B_W_down)
    return x
```

```cpp
#include <hip/hip_runtime.h>
#include <hip/hip_bf16.h>

#define LAS __attribute__((address_space(3)))
#define GAS __attribute__((address_space(1)))
typedef __bf16 bf16x8 __attribute__((ext_vector_type(8)));
typedef __bf16 bf16x4 __attribute__((ext_vector_type(4)));
typedef __bf16 bf16x2 __attribute__((ext_vector_type(2)));
typedef float f32x4 __attribute__((ext_vector_type(4)));
typedef float f32x2 __attribute__((ext_vector_type(2)));
typedef unsigned u32x4 __attribute__((ext_vector_type(4)));
typedef unsigned u32x2 __attribute__((ext_vector_type(2)));
typedef LAS char* lptr;
typedef const GAS char* gcptr;
typedef GAS char* gptr;

#define DI __device__ __forceinline__
#define WAIT_V(n) asm volatile("s_waitcnt vmcnt(" #n ")" ::: "memory")
#define WAIT_L0() asm volatile("s_waitcnt lgkmcnt(0)" ::: "memory")
#define CFENCE() asm volatile("" ::: "memory")
#define BAR() do { CFENCE(); __builtin_amdgcn_s_barrier(); CFENCE(); } while (0)
#define SCHED() __builtin_amdgcn_sched_barrier(0)

constexpr int S_ = 2048, D_ = 576, F_ = 4608;
constexpr float LN_EPS = 1e-5f;
constexpr size_t ACT_BYTES = (size_t)S_ * D_ * 2;
constexpr unsigned WFF_BYTES = (unsigned)D_ * F_ * 2;

DI void glds16(gcptr g, lptr l) {
    __builtin_amdgcn_global_load_lds((const GAS void*)g, (LAS void*)l, 16, 0, 0);
}
typedef __amdgpu_buffer_rsrc_t rsrc_t;
DI rsrc_t mk_rsrc(const void* p, unsigned bytes) { return __builtin_amdgcn_make_buffer_rsrc((void*)p, 0, bytes, 0x00020000); }
DI void blds16(rsrc_t r, unsigned voff, unsigned soff, lptr l) {
    __builtin_amdgcn_raw_ptr_buffer_load_lds(r, (LAS void*)l, 16, voff, soff, 0, 0);
}
DI float relu(float x) { return __builtin_amdgcn_fmed3f(x, 0.0f, __builtin_inff()); }
DI bf16x8 lds_frag(lptr p) { return *(const LAS bf16x8*)p; }
DI bf16x8 lds_tr2(lptr p0, lptr p1) {
    bf16x4 lo = __builtin_amdgcn_ds_read_tr16_b64_v4bf16((LAS bf16x4*)p0);
    bf16x4 hi = __builtin_amdgcn_ds_read_tr16_b64_v4bf16((LAS bf16x4*)p1);
    return __builtin_shufflevector(lo, hi, 0, 1, 2, 3, 4, 5, 6, 7);
}
DI f32x4 mfma16(bf16x8 a, bf16x8 b, f32x4 c) { return __builtin_amdgcn_mfma_f32_16x16x32_bf16(a, b, c, 0, 0, 0); }
DI unsigned pack2(float a, float b) {
    f32x2 f = {a, b};
    bf16x2 h = __builtin_convertvector(f, bf16x2);
    return __builtin_bit_cast(unsigned, h);
}
DI float bf_lo(unsigned u) { return __uint_as_float(u << 16); }
DI float bf_hi(unsigned u) { return __uint_as_float(u & 0xffff0000u); }
DI float dot2(unsigned a, unsigned b, float c) {
    return __builtin_amdgcn_fdot2_f32_bf16(__builtin_bit_cast(bf16x2, a), __builtin_bit_cast(bf16x2, b), c, false);
}
DI float xor32(float v) {
    unsigned u = __float_as_uint(v);
    auto r = __builtin_amdgcn_permlane32_swap(u, u, false, false);
    return __uint_as_float((threadIdx.x & 32) ? r[0] : r[1]);
}

constexpr int A_IMG = 0;
constexpr int A_RING = 92160;
constexpr int A_QKV_RS = 784;
constexpr int A_O = 80 * A_QKV_RS;
constexpr int A_O_RS = 160;
constexpr int A_LDS = 163840;

__global__ __launch_bounds__(512, 2) void kA(const char* __restrict__ x_, const char* __restrict__ wqkv_, char* __restrict__ ot_,
                                             const char* __restrict__ wg_, const char* __restrict__ wu_, float* __restrict__ cs_) {
    extern __shared__ __attribute__((aligned(16))) char smem_[];
    lptr sm = (lptr)smem_;
    gcptr x = (gcptr)x_, wqkv = (gcptr)wqkv_;
    const int tid = threadIdx.x, lane = tid & 63;
    const int w = __builtin_amdgcn_readfirstlane(tid >> 6);
    const int bid = blockIdx.x;
    const int tt = (bid & 7) * 4 + ((bid >> 3) & 3), hg = bid >> 5;
    const int t = lane & 15, g = lane >> 4;

    const int lr0 = 8 * w + (lane >> 3), ca = lane & 7;
    u32x4 xr[2][9];
    {
        int gr = tt * 64 - 16 + lr0; gr = gr < 0 ? 0 : gr;
        gcptr p = x + (size_t)gr * 1152 + ca * 16;
#pragma unroll
        for (int j = 0; j < 9; ++j) xr[0][j] = *(const GAS u32x4*)(p + j * 128);
        if (w < 2) {
            gcptr p2 = x + (size_t)(tt * 64 + 48 + lr0) * 1152 + ca * 16;
#pragma unroll
            for (int j = 0; j < 9; ++j) xr[1][j] = *(const GAS u32x4*)(p2 + j * 128);
        }
    }
    unsigned woff[2];
    lptr ring = sm + A_RING + (w < 7 ? w : 0) * 10240;
#pragma unroll
    for (int i = 0; i < 2; ++i) {
        const int p = i * 16 + (lane >> 2);
        int c = 4 * w + ((lane & 3) ^ (((p >> 3) & 1) << 1));
        c = c > 26 ? 26 : c;
        const int mat = c / 9, cc = c - mat * 9;
        woff[i] = (unsigned)(p * 3456 + mat * 1152 + hg * 144 + cc * 16);
    }
    if (w < 7) {
#pragma unroll
        for (int s = 0; s < 4; ++s) {
            glds16(wqkv + (size_t)s * 32 * 3456 + woff[0], ring + s * 2048);
            glds16(wqkv + (size_t)s * 32 * 3456 + woff[1], ring + s * 2048 + 1024);
        }
    }
#pragma unroll
    for (int rep = 0; rep < 2; ++rep) {
        if (rep == 1 && w >= 2) break;
        const int lr = lr0 + rep * 64;
        float s = 0.f;
#pragma unroll
        for (int j = 0; j < 9; ++j)
#pragma unroll
            for (int e = 0; e < 4; ++e) s += bf_lo(xr[rep][j][e]) + bf_hi(xr[rep][j][e]);
        s += __shfl_xor(s, 1); s += __shfl_xor(s, 2); s += __shfl_xor(s, 4);
        const float mean = s * (1.0f / D_);
        float q = 0.f;
#pragma unroll
        for (int j = 0; j < 9; ++j)
#pragma unroll
            for (int e = 0; e < 4; ++e) {
                const float a = bf_lo(xr[rep][j][e]) - mean, b = bf_hi(xr[rep][j][e]) - mean;
                q += a * a + b * b;
            }
        q += __shfl_xor(q, 1); q += __shfl_xor(q, 2); q += __shfl_xor(q, 4);
        const float rstd = 1.0f / sqrtf(q * (1.0f / D_) + LN_EPS);
        lptr dst = sm + A_IMG + ((ca >> 2) * 5 + (lr >> 4)) * 1024 + (ca & 3) * 256 + (((lr & 15) ^ ca) * 16);
#pragma unroll
        for (int j = 0; j < 9; ++j) {
            u32x4 o;
#pragma unroll
            for (int e = 0; e < 4; ++e) o[e] = pack2((bf_lo(xr[rep][j][e]) - mean) * rstd, (bf_hi(xr[rep][j][e]) - mean) * rstd);
            *(LAS u32x4*)(dst + j * 10240) = o;
        }
    }
    WAIT_L0();
    BAR();

    f32x4 acc[2][5];
#pragma unroll
    for (int j = 0; j < 2; ++j)
#pragma unroll
        for (int i = 0; i < 5; ++i) acc[j][i] = (f32x4){0.f, 0.f, 0.f, 0.f};

    if (w < 7) {
        const int q = (lane >> 2) & 3, pp = lane & 3;
        lptr ta0 = sm + A_IMG + g * 256 + ((t ^ g) * 16);
        lptr ta1 = sm + A_IMG + g * 256 + ((t ^ g ^ 4) * 16) + 5120;
        lptr wb0 = ring + (8 * g + q) * 64 + ((g & 1) * 32) + pp * 8;
        lptr wb1 = ring + (8 * g + q) * 64 + (((g & 1) ^ 1) * 32) + pp * 8;
#pragma unroll
        for (int s = 0; s < 18; ++s) {
            if (s <= 14) { WAIT_V(6); } else if (s == 15) { WAIT_V(4); } else if (s == 16) { WAIT_V(2); } else { WAIT_V(0); }
            const int slot = (s % 5) * 2048;
            bf16x8 wf0 = lds_tr2(wb0 + slot, wb0 + slot + 256);
            bf16x8 wf1 = lds_tr2(wb1 + slot, wb1 + slot + 256);
            bf16x8 tk[5];
            lptr ta = ((s & 1) ? ta1 : ta0) + (s >> 1) * 10240;
#pragma unroll
            for (int i = 0; i < 5; ++i) tk[i] = lds_frag(ta + i * 1024);
            if (s + 4 < 18) {
                const int ns = ((s + 4) % 5) * 2048;
                glds16(wqkv + (size_t)(s + 4) * 32 * 3456 + woff[0], ring + ns);
                glds16(wqkv + (size_t)(s + 4) * 32 * 3456 + woff[1], ring + ns + 1024);
            }
#pragma unroll
            for (int i = 0; i < 5; ++i) {
                acc[0][i] = mfma16(wf0, tk[i], acc[0][i]);
                acc[1][i] = mfma16(wf1, tk[i], acc[1][i]);
            }
        }
    } else if (cs_ != nullptr && bid < 144) {
        gcptr wsrc = (gcptr)(bid < 72 ? wg_ : wu_);
        const int cb = bid < 72 ? bid : bid - 72;
        gcptr p = wsrc + (size_t)(lane >> 3) * 9216 + cb * 128 + (lane & 7) * 16;
        float a[8];
#pragma unroll
        for (int e = 0; e < 8; ++e) a[e] = 0.f;
        for (int it = 0; it < 4; ++it) {
            u32x4 v[18];
#pragma unroll
            for (int j = 0; j < 18; ++j) v[j] = *(const GAS u32x4*)(p + (size_t)(it * 18 + j) * 8 * 9216);
#pragma unroll
            for (int j = 0; j < 18; ++j)
#pragma unroll
                for (int e = 0; e < 4; ++e) { a[2 * e] += bf_lo(v[j][e]); a[2 * e + 1] += bf_hi(v[j][e]); }
        }
#pragma unroll
        for (int e = 0; e < 8; ++e) {
            a[e] += __shfl_xor(a[e], 8); a[e] += __shfl_xor(a[e], 16); a[e] += __shfl_xor(a[e], 32);
        }
        if (lane < 8) {
            float* dst = cs_ + bid * 64 + lane * 8;
            *(f32x4*)dst = (f32x4){a[0], a[1], a[2], a[3]};
            *(f32x4*)(dst + 4) = (f32x4){a[4], a[5], a[6], a[7]};
        }
    }
    WAIT_L0();
    BAR();
    if (w < 7) {
#pragma unroll
        for (int j = 0; j < 2; ++j) {
            const int c0 = 32 * w + 16 * j + 4 * g, c1 = c0 + 2;
            if (c0 < 216) {
                const int o0 = (c0 / 18) * 64 + (c0 % 18) * 2, o1 = (c1 / 18) * 64 + (c1 % 18) * 2;
#pragma unroll
                for (int i = 0; i < 5; ++i) {
                    lptr row = sm + (16 * i + t) * A_QKV_RS;
                    *(LAS unsigned*)(row + o0) = pack2(acc[j][i][0], acc[j][i][1]);
                    *(LAS unsigned*)(row + o1) = pack2(acc[j][i][2], acc[j][i][3]);
                }
            }
        }
    }
    WAIT_L0();
    BAR();
    {
        const int h = lane & 3, sl = 8 * w + ((lane >> 2) & 7), e = lane >> 5;
        const int pos = tt * 64 + sl;
        lptr qp = sm + (16 + sl) * A_QKV_RS + h * 64;
        const u32x4 q0 = *(const LAS u32x4*)qp, q1 = *(const LAS u32x4*)(qp + 16);
        const unsigned q2 = *(const LAS unsigned*)(qp + 32);
        lptr kp = sm + (16 + sl - 8 * e) * A_QKV_RS + 256 + h * 64;
        float lg[4];
#pragma unroll
        for (int u = 0; u < 4; ++u) {
            lptr p = kp - 2 * u * A_QKV_RS;
            const u32x4 k0 = *(const LAS u32x4*)p, k1 = *(const LAS u32x4*)(p + 16);
            const unsigned k2 = *(const LAS unsigned*)(p + 32);
            float d = dot2(q2, k2, 0.f);
#pragma unroll
            for (int c = 0; c < 4; ++c) { d = dot2(q0[c], k0[c], d); d = dot2(q1[c], k1[c], d); }
            d *= 0.23570226039551584f;
            lg[u] = (pos - 2 * (4 * e + u) >= 0) ? d : -1e9f;
        }
        float mx = fmaxf(fmaxf(lg[0], lg[1]), fmaxf(lg[2], lg[3]));
        mx = fmaxf(mx, xor32(mx));
        float pw[4], sum = 0.f;
#pragma unroll
        for (int u = 0; u < 4; ++u) { pw[u] = __expf(lg[u] - mx); sum += pw[u]; }
        sum += xor32(sum);
        const float inv = 1.0f / sum;
        float o[18];
#pragma unroll
        for (int c = 0; c < 18; ++c) o[c] = 0.f;
#pragma unroll
        for (int u = 0; u < 4; ++u) {
            lptr p = kp + 256 - 2 * u * A_QKV_RS;
            const u32x4 v0 = *(const LAS u32x4*)p, v1 = *(const LAS u32x4*)(p + 16);
            const unsigned v2 = *(const LAS unsigned*)(p + 32);
            const float pu = pw[u] * inv;
#pragma unroll
            for (int c = 0; c < 4; ++c) {
                o[2 * c] += pu * bf_lo(v0[c]); o[2 * c + 1] += pu * bf_hi(v0[c]);
                o[8 + 2 * c] += pu * bf_lo(v1[c]); o[9 + 2 * c] += pu * bf_hi(v1[c]);
            }
            o[16] += pu * bf_lo(v2); o[17] += pu * bf_hi(v2);
        }
        lptr op = sm + A_O + sl * A_O_RS + h * 36 + e * 16;
#pragma unroll
        for (int c = 0; c < 5; ++c) {
            auto r0 = __builtin_amdgcn_permlane32_swap(__float_as_uint(o[2 * c]), __float_as_uint(o[8 + 2 * c]), false, false);
            auto r1 = __builtin_amdgcn_permlane32_swap(__float_as_uint(o[2 * c + 1]), __float_as_uint(o[9 + 2 * c]), false, false);
            const float a = __uint_as_float(r0[0]) + __uint_as_float(r0[1]);
            const float b = __uint_as_float(r1[0]) + __uint_as_float(r1[1]);
            *(LAS unsigned*)(op + c * 4) = pack2(a, b);
        }
    }
    WAIT_L0();
    BAR();
    for (int pc = tid; pc < 576; pc += 512) {
        const int c = pc >> 6, sl = pc & 63;
        const u32x4 v = *(const LAS u32x4*)(sm + A_O + sl * A_O_RS + c * 16);
        const int cc = hg * 9 + c;
        *(GAS u32x4*)((gptr)ot_ + (size_t)(((cc >> 2) * 128 + tt * 4 + (sl >> 4)) * 1024 + (cc & 3) * 256 + (sl & 15) * 16)) = v;
    }
}

constexpr int B_RED = 92160;
constexpr int B_LDS = 92160 + 20480;

__global__ __launch_bounds__(512, 2) void kB(const char* __restrict__ ot_, const char* __restrict__ wo_, const char* __restrict__ x_,
                                             char* __restrict__ x1t_, float* __restrict__ stats_) {
    extern __shared__ __attribute__((aligned(16))) char smem_[];
    lptr sm = (lptr)smem_;
    const int tid = threadIdx.x, lane = tid & 63;
    const int w = __builtin_amdgcn_readfirstlane(tid >> 6);
    const int bid = blockIdx.x;
    const int tt = (bid & 7) * 4 + ((bid >> 3) & 3), cb = bid >> 5;
    const int t = lane & 15, g = lane >> 4;
    const int ti = w & 3, kh = w >> 2;
    const int token = tt * 64 + ti * 16 + t;

    bf16x8 of[9];
    {
        gcptr p = (gcptr)ot_ + (size_t)((kh * 9) * 128 + tt * 4 + ti) * 1024 + lane * 16;
#pragma unroll
        for (int s = 0; s < 9; ++s) of[s] = *(const GAS bf16x8*)(p + (size_t)s * 131072);
    }
    u32x2 res[5];
    {
        gcptr p = (gcptr)x_ + (size_t)token * 1152 + cb * 144 + g * 8;
#pragma unroll
        for (int j = 0; j < 4; ++j) res[j] = *(const GAS u32x2*)(p + j * 32);
        res[4] = *(const GAS u32x2*)(p + ((g < 2) ? 128 : 0));
    }
    {
        gcptr wo = (gcptr)wo_ + cb * 144;
#pragma unroll
        for (int i = 0; i < 12; ++i) {
            const int ins = i * 8 + w;
            if (ins < 90) {
                const int qq = ins * 64 + lane;
                const int p = qq / 10; int c = qq - p * 10; c = c > 8 ? 8 : c;
                const int r = p ^ (((p >> 3) & 1) << 2);
                glds16(wo + (unsigned)(r * 1152 + c * 16), sm + ins * 1024);
            }
        }
    }
    WAIT_V(0);
    BAR();
    f32x4 acc[5];
#pragma unroll
    for (int j = 0; j < 5; ++j) acc[j] = (f32x4){0.f, 0.f, 0.f, 0.f};
    {
        const int q = (lane >> 2) & 3, pp = lane & 3;
        lptr b0 = sm + kh * 46080 + (8 * g + 4 * (g & 1) + q) * 160 + pp * 8;
        lptr b1 = sm + kh * 46080 + (8 * g + 4 * (1 - (g & 1)) + q) * 160 + pp * 8;
#pragma unroll
        for (int s = 0; s < 9; ++s)
#pragma unroll
            for (int j = 0; j < 5; ++j) {
                const bf16x8 wf = lds_tr2(b0 + s * 5120 + j * 32, b1 + s * 5120 + j * 32);
                acc[j] = mfma16(wf, of[s], acc[j]);
            }
    }
    if (kh == 1) {
#pragma unroll
        for (int j = 0; j < 5; ++j) *(LAS f32x4*)(sm + B_RED + (ti * 5 + j) * 1024 + lane * 16) = acc[j];
    }
    WAIT_L0();
    BAR();
    if (kh == 0) {
        float v[5][4];
        float s = 0.f;
#pragma unroll
        for (int j = 0; j < 5; ++j) {
            const f32x4 o = *(const LAS f32x4*)(sm + B_RED + (ti * 5 + j) * 1024 + lane * 16);
            const f32x4 a = acc[j] + o;
            u32x2 pk;
            pk[0] = pack2(a[0] + bf_lo(res[j][0]), a[1] + bf_hi(res[j][0]));
            pk[1] = pack2(a[2] + bf_lo(res[j][1]), a[3] + bf_hi(res[j][1]));
            const bool ok = (j < 4) || (g < 2);
            v[j][0] = bf_lo(pk[0]); v[j][1] = bf_hi(pk[0]); v[j][2] = bf_lo(pk[1]); v[j][3] = bf_hi(pk[1]);
            if (ok) {
                s += (v[j][0] + v[j][1]) + (v[j][2] + v[j][3]);
                const int d = cb * 72 + 16 * j + 4 * g;
                *(GAS u32x2*)((gptr)x1t_ + (size_t)(((d >> 5) * 128 + tt * 4 + ti) * 1024 + ((d >> 3) & 3) * 256 + t * 16 + (d & 7) * 2)) = pk;
            }
        }
        s += __shfl_xor(s, 16); s += __shfl_xor(s, 32);
        const float mean = s * (1.0f / 72.0f);
        float q = 0.f;
#pragma unroll
        for (int j = 0; j < 5; ++j) {
            const bool ok = (j < 4) || (g < 2);
#pragma unroll
            for (int r = 0; r < 4; ++r) { const float d = v[j][r] - mean; q += ok ? d * d : 0.f; }
        }
        q += __shfl_xor(q, 16); q += __shfl_xor(q, 32);
        if (g == 0) *(f32x2*)(stats_ + ((size_t)token * 8 + cb) * 2) = (f32x2){mean, q};
    }
}

constexpr int C_BUF = 45056;
constexpr int C_H = 90112;
constexpr int C_LDS = 163840;

template <int M>
DI void c_body(lptr sm, const char* x1t_, const float* __restrict__ stats_, const float* __restrict__ cs_, const char* wg_, const char* wu_,
               const char* wd_, gptr part, const int n, const int fs, const int T) {
    constexpr int m = M;
    int lane = threadIdx.x & 63;
    asm volatile("" : "+v"(lane));
    const int w = m * 4 + n;
    const int t = lane & 15, g = lane >> 4;
    const int q = (lane >> 2) & 3, pp = lane & 3;
    const rsrc_t rx = mk_rsrc(x1t_, ACT_BYTES), rg = mk_rsrc(wg_, WFF_BYTES), ru = mk_rsrc(wu_, WFF_BYTES), rd = mk_rsrc(wd_, WFF_BYTES);
    const rsrc_t rs = mk_rsrc((n & 1) ? wu_ : wg_, WFF_BYTES);

    unsigned so[3];
    const int i0 = (M == 0 ? 8 : 0) + 2 * n;
#pragma unroll
    for (int k = 0; k < 3; ++k) {
        const int ins = (k < 2) ? i0 + k : 16 + (n >> 1);
        const int qq = ins * 64 + lane;
        const int p = qq / 36, c = qq - p * 36;
        so[k] = (unsigned)(p * 9216 + (c ^ (((p >> 3) & 1) << 1)) * 16);
    }
    const int xdst = (n & 1) * 18432 + (16 + (n >> 1)) * 1024;
    const unsigned l16 = lane * 16;
    const unsigned sd = (unsigned)((lane >> 3) * 1152 + ((lane & 7) ^ (((lane >> 4) & 3) << 1)) * 16);

#define C_STAGE1(s, buf) do { \
        const unsigned ko_ = (unsigned)(s) * (32 * 9216) + fs * 576; lptr b_ = sm + (buf); \
        blds16(rx, l16, (unsigned)(s) * 131072 + (T * 8 + w) * 1024, b_ + 36864 + w * 1024); \
        blds16(rg, so[0], ko_, b_ + i0 * 1024); \
        blds16(ru, so[0], ko_, b_ + 18432 + i0 * 1024); \
        blds16(rg, so[1], ko_, b_ + (i0 + 1) * 1024); \
        blds16(ru, so[1], ko_, b_ + 18432 + (i0 + 1) * 1024); \
        if (M == 0) blds16(rs, so[2], ko_, b_ + xdst); \
    } while (0)
#define C_STAGE2(b, buf) do { \
        lptr b_ = sm + (buf); \
        _Pragma("unroll") for (int k_ = 0; k_ < (M == 0 ? 5 : 4); ++k_) { \
            const int ins_ = (M == 0 ? 16 + 5 * n : 4 * n) + k_; \
            blds16(rd, sd, (unsigned)((fs * 288 + ins_ * 8) * 1152 + (b) * 128), b_ + ins_ * 1024); } \
        CFENCE(); \
    } while (0)

    f32x4 st[4], csv[9];
#pragma unroll
    for (int i = 0; i < 4; ++i) st[i] = *(const f32x4*)(stats_ + ((size_t)(T * 128 + m * 64 + i * 16 + t) * 8 + g * 2) * 2);
#pragma unroll
    for (int j = 0; j < 4; ++j) {
        csv[j] = *(const f32x4*)(cs_ + fs * 288 + (4 * n + j) * 16 + 4 * g);
        csv[4 + j] = *(const f32x4*)(cs_ + F_ + fs * 288 + (4 * n + j) * 16 + 4 * g);
    }
    csv[8] = *(const f32x4*)(cs_ + (g >> 1) * F_ + fs * 288 + 256 + 8 * n + 4 * (g & 1));
    C_STAGE1(0, 0);
    C_STAGE1(1, C_BUF);

    f32x4 acc[9][4];
    float rs2[4];
#pragma unroll
    for (int i = 0; i < 4; ++i) {
        float ms = st[i][0] + st[i][2];
        ms += __shfl_xor(ms, 16); ms += __shfl_xor(ms, 32);
        const float mean = ms * 0.125f;
        const float d0 = st[i][0] - mean, d1 = st[i][2] - mean;
        float m2 = st[i][1] + st[i][3] + 72.0f * (d0 * d0 + d1 * d1);
        m2 += __shfl_xor(m2, 16); m2 += __shfl_xor(m2, 32);
        rs2[i] = 1.0f / (m2 * (1.0f / D_) + LN_EPS);
#pragma unroll
        for (int j = 0; j < 9; ++j) acc[j][i] = csv[j] * (-mean);
    }

    const int rowoff = (8 * g + q) * 576 + pp * 8;
    const int be = rowoff + 128 * n + 32 * (g & 1);
    const int bo = rowoff + 128 * n - 32 * (g & 1);
    const int bm = (8 * g + q) * 576 + (((32 + n) ^ (2 * (g & 1))) * 16) + 8 * (pp & 1) + (pp >> 1) * 18432;
    const int bt = 36864 + m * 4096 + lane * 16;

#define C_STEP1(cur, STAGE, NX, NY) do { \
        bf16x8 tk[4], wf[9]; \
        lptr b = sm + (cur); \
        _Pragma("unroll") for (int i = 0; i < 4; ++i) tk[i] = lds_frag(b + bt + i * 1024); \
        _Pragma("unroll") for (int j = 0; j < 4; ++j) { \
            lptr p = b + ((j & 1) ? bo : be) + 32 * j; \
            wf[j] = lds_tr2(p, p + 2304); \
            wf[4 + j] = lds_tr2(p + 18432, p + 18432 + 2304); } \
        wf[8] = lds_tr2(b + bm, b + bm + 2304); \
        STAGE; \
        if (M == 1) WAIT_V(NY); \
        WAIT_L0(); SCHED(); BAR(); \
        __builtin_amdgcn_s_setprio(1); \
        _Pragma("unroll") for (int j = 0; j < 9; ++j) \
            _Pragma("unroll") for (int i = 0; i < 4; ++i) acc[j][i] = mfma16(wf[j], tk[i], acc[j][i]); \
        __builtin_amdgcn_s_setprio(0); \
        if (M == 0) WAIT_V(NX); \
        SCHED(); BAR(); \
    } while (0)

    SCHED();
    WAIT_V(0);
    BAR();
    if (M == 1) BAR();
    int cur = 0;
    for (int s = 0; s < 16; ++s) {
        const int nxt2 = (cur == 0) ? 2 * C_BUF : cur - C_BUF;
        C_STEP1(cur, C_STAGE1(s + 2, nxt2), 6, 5);
        cur = (cur == 2 * C_BUF) ? 0 : cur + C_BUF;
    }
    C_STEP1(C_BUF, C_STAGE2(0, 0), 5, 4);
    C_STEP1(2 * C_BUF, C_STAGE2(1, C_BUF), 5, 4);

    {
        lptr hb = sm + C_H + m * 36864;
#pragma unroll
        for (int i = 0; i < 4; ++i)
#pragma unroll
            for (int a = 0; a < 2; ++a) {
                u32x4 o;
#pragma unroll
                for (int k = 0; k < 2; ++k) {
                    const f32x4 gt = acc[2 * a + k][i], up = acc[4 + 2 * a + k][i];
                    float hv[4];
#pragma unroll
                    for (int r = 0; r < 4; ++r) hv[r] = relu(gt[r]) * (up[r] * rs2[i]);
                    o[2 * k] = pack2(hv[0], hv[1]); o[2 * k + 1] = pack2(hv[2], hv[3]);
                }
                *(LAS u32x4*)(hb + (i * 9 + 2 * n + a) * 1024 + lane * 16) = o;
            }
#pragma unroll
        for (int i = 0; i < 4; i += 2) {
            float hv[4];
#pragma unroll
            for (int r = 0; r < 4; ++r) {
                const float xa = acc[8][i][r] * ((lane & 32) ? rs2[i] : 1.0f), xb = acc[8][i + 1][r] * ((lane & 32) ? rs2[i + 1] : 1.0f);
                auto x = __builtin_amdgcn_permlane32_swap(__float_as_uint(xa), __float_as_uint(xb), false, false);
                hv[r] = relu(__uint_as_float(x[0])) * __uint_as_float(x[1]);
            }
            u32x2 o; o[0] = pack2(hv[0], hv[1]); o[1] = pack2(hv[2], hv[3]);
            *(LAS u32x2*)(hb + ((i + (g >> 1)) * 9 + 8) * 1024 + (2 * (n & 1) + (g & 1)) * 256 + t * 16 + (n >> 1) * 8) = o;
        }
    }
    WAIT_V(0);
    WAIT_L0();
    SCHED();
    BAR();
    bf16x8 hf[4][9];
    const int bd = (4 * g + q) * 128 + ((n ^ ((2 * g + (q >> 1)) & 3)) * 32) + pp * 8;
    gptr pout = part + (size_t)fs * ACT_BYTES + (size_t)(((T * 2 + m) * 8 + (t >> 2)) * 36 + n) * 256 + g * 64 + (t & 3) * 16;
    f32x4 a2[4];
#define C_SLOT(b) (((b) & 2) ? C_H + ((b) & 1) * 36864 : ((b) & 1) * C_BUF)
#define C_STORE(b) do { \
        _Pragma("unroll") for (int ip = 0; ip < 2; ++ip) { \
            u32x4 o; \
            o[0] = pack2(a2[2 * ip][0], a2[2 * ip][1]); o[1] = pack2(a2[2 * ip][2], a2[2 * ip][3]); \
            o[2] = pack2(a2[2 * ip + 1][0], a2[2 * ip + 1][1]); o[3] = pack2(a2[2 * ip + 1][2], a2[2 * ip + 1][3]); \
            *(GAS u32x4*)(pout + ip * 36864 + (b) * 1024) = o; } \
        CFENCE(); \
    } while (0)
#pragma unroll
    for (int b = 0; b < 9; ++b) {
        bf16x8 wf[9];
#pragma unroll
        for (int s = 0; s < 9; ++s) wf[s] = lds_tr2(sm + C_SLOT(b) + bd + s * 4096, sm + C_SLOT(b) + bd + s * 4096 + 2048);
        if (b == 0) {
            lptr hb = sm + C_H + m * 36864 + lane * 16;
#pragma unroll
            for (int s = 0; s < 9; ++s)
#pragma unroll
                for (int i = 0; i < 4; ++i) hf[i][s] = lds_frag(hb + (i * 9 + s) * 1024);
            if (M == 1) C_STAGE2(2, C_SLOT(2));
        } else {
            C_STORE(b - 1);
            if (b == 1) C_STAGE2(3, C_SLOT(3));
            if (b + 3 < 9) C_STAGE2(b + 3, C_SLOT(b + 3));
        }
        if (M == 1) {
            if (b == 1 || b == 2) WAIT_V(10); else if (b >= 3 && b <= 5) WAIT_V(12); else if (b == 6) WAIT_V(8); else if (b == 7) WAIT_V(4);
        }
        WAIT_L0();
        SCHED();
        BAR();
        if (M == 0 && b == 0) C_STAGE2(2, C_SLOT(2));
        __builtin_amdgcn_s_setprio(1);
#pragma unroll
        for (int i = 0; i < 4; ++i) a2[i] = mfma16(wf[0], hf[i][0], (f32x4){0.f, 0.f, 0.f, 0.f});
#pragma unroll
        for (int s = 1; s < 9; ++s)
#pragma unroll
            for (int i = 0; i < 4; ++i) a2[i] = mfma16(wf[s], hf[i][s], a2[i]);
        __builtin_amdgcn_s_setprio(0);
        if (M == 0) {
            if (b == 1 || b == 2) WAIT_V(12); else if (b >= 3 && b <= 5) WAIT_V(14); else if (b == 6) WAIT_V(9); else if (b == 7) WAIT_V(4);
        }
        SCHED();
        if (!(M == 1 && b == 8)) BAR();
    }
    C_STORE(8);
#undef C_STAGE1
#undef C_STAGE2
#undef C_STEP1
#undef C_SLOT
#undef C_STORE
}

__global__ __launch_bounds__(512, 2) void kC(const char* __restrict__ x1t_, const float* __restrict__ stats_, const float* __restrict__ cs_,
                                             const char* __restrict__ wg_, const char* __restrict__ wu_, const char* __restrict__ wd_,
                                             char* __restrict__ part_) {
    extern __shared__ __attribute__((aligned(16))) char smem_[];
    lptr sm = (lptr)smem_;
    const int w = __builtin_amdgcn_readfirstlane(threadIdx.x >> 6);
    const int bid = blockIdx.x;
    const int fs = 2 * (bid & 7) + ((bid >> 3) & 1), T = bid >> 4;
    if (w < 4) c_body<0>(sm, x1t_, stats_, cs_, wg_, wu_, wd_, (gptr)part_, w & 3, fs, T);
    else       c_body<1>(sm, x1t_, stats_, cs_, wg_, wu_, wd_, (gptr)part_, w & 3, fs, T);
}

__global__ __launch_bounds__(576) void kE(const char* __restrict__ part_, const char* __restrict__ x1t_, char* __restrict__ out_) {
    const int tid = threadIdx.x, bid = blockIdx.x;
    const int R = (bid & 7) * 32 + (bid >> 3);
    gcptr p = (gcptr)part_ + (size_t)R * 9216 + tid * 16;
    u32x4 v[16];
#pragma unroll
    for (int f = 0; f < 16; ++f) v[f] = *(const GAS u32x4*)(p + (size_t)f * ACT_BYTES);
    const int d0 = (tid >> 2) * 4;
    const int ta = (R >> 2) * 32 + (R & 3) * 4 + (tid & 3);
    gcptr xp = (gcptr)x1t_ + (size_t)(((d0 >> 5) * 128 + (ta >> 4)) * 1024 + ((d0 >> 3) & 3) * 256 + (ta & 15) * 16 + (d0 & 7) * 2);
    const u32x2 ra = *(const GAS u32x2*)xp, rb = *(const GAS u32x2*)(xp + 1024);
    float a[8];
#pragma unroll
    for (int e = 0; e < 8; ++e) a[e] = 0.f;
#pragma unroll
    for (int f = 0; f < 16; ++f)
#pragma unroll
        for (int e = 0; e < 4; ++e) { a[2 * e] += bf_lo(v[f][e]); a[2 * e + 1] += bf_hi(v[f][e]); }
    u32x2 oa, ob;
    oa[0] = pack2(a[0] + bf_lo(ra[0]), a[1] + bf_hi(ra[0])); oa[1] = pack2(a[2] + bf_lo(ra[1]), a[3] + bf_hi(ra[1]));
    ob[0] = pack2(a[4] + bf_lo(rb[0]), a[5] + bf_hi(rb[0])); ob[1] = pack2(a[6] + bf_lo(rb[1]), a[7] + bf_hi(rb[1]));
    gptr op = (gptr)out_ + (size_t)ta * 1152 + d0 * 2;
    *(GAS u32x2*)op = oa;
    *(GAS u32x2*)(op + 16 * 1152) = ob;
}

extern "C" void kernel_launch(void* const* d_in, const int* in_sizes, int n_in, void* d_out, int out_size, void* d_ws, size_t ws_size,
                              hipStream_t stream) {
    (void)in_sizes; (void)n_in; (void)out_size; (void)ws_size;
    const char* x0 = (const char*)d_in[0];
    const char* wqkv = (const char*)d_in[1];
    const char* wo = (const char*)d_in[2];
    const char* wg = (const char*)d_in[4];
    const char* wu = (const char*)d_in[5];
    const char* wd = (const char*)d_in[6];
    char* ws = (char*)d_ws;
    char* ot = ws;
    char* x1t = ws + ACT_BYTES;
    char* x2 = ws + 2 * ACT_BYTES;
    float* stats = (float*)(ws + 3 * ACT_BYTES);
    float* cs = (float*)(ws + 3 * ACT_BYTES + 131072);
    char* part = ws + 3 * ACT_BYTES + 131072 + 65536;

    (void)hipFuncSetAttribute((const void*)kA, hipFuncAttributeMaxDynamicSharedMemorySize, A_LDS);
    (void)hipFuncSetAttribute((const void*)kB, hipFuncAttributeMaxDynamicSharedMemorySize, B_LDS);
    (void)hipFuncSetAttribute((const void*)kC, hipFuncAttributeMaxDynamicSharedMemorySize, C_LDS);

    for (int l = 0; l < 2; ++l) {
        const char* xin = l == 0 ? x0 : x2;
        char* xout = l == 0 ? x2 : (char*)d_out;
        kA<<<256, 512, A_LDS, stream>>>(xin, wqkv, ot, wg, wu, l == 0 ? cs : nullptr);
        kB<<<256, 512, B_LDS, stream>>>(ot, wo, xin, x1t, stats);
        kC<<<256, 512, C_LDS, stream>>>(x1t, stats, cs, wg, wu, wd, part);
        kE<<<256, 576, 0, stream>>>(part, x1t, xout);
    }
}
```

```cpp
#include <hip/hip_runtime.h>
#include <hip/hip_bf16.h>

#define LAS __attribute__((address_space(3)))
#define GAS __attribute__((address_space(1)))
typedef __bf16 bf16x8 __attribute__((ext_vector_type(8)));
typedef __bf16 bf16x4 __attribute__((ext_vector_type(4)));
typedef __bf16 bf16x2 __attribute__((ext_vector_type(2)));
typedef float f32x4 __attribute__((ext_vector_type(4)));
typedef float f32x2 __attribute__((ext_vector_type(2)));
typedef unsigned u32x4 __attribute__((ext_vector_type(4)));
typedef unsigned u32x2 __attribute__((ext_vector_type(2)));
typedef unsigned long long u64;
typedef LAS char* lptr;
typedef __amdgpu_buffer_rsrc_t rsrc_t;

#define DI __device__ __forceinline__
#define WAIT_V(n) asm volatile("s_waitcnt vmcnt(" #n ")" ::: "memory")
#define WAIT_L0() asm volatile("s_waitcnt lgkmcnt(0)" ::: "memory")
#define CFENCE() asm volatile("" ::: "memory")
#define BAR() do { CFENCE(); __builtin_amdgcn_s_barrier(); CFENCE(); } while (0)
#define SCHED() __builtin_amdgcn_sched_barrier(0)
#define SC1 16

#define STAMP(i)
#define RSTAMP(v)
#define RFLUSH(v, i)
#define RDECL(v)

constexpr int S_ = 2048, D_ = 576, F_ = 4608;
constexpr float LN_EPS = 1e-5f;
constexpr unsigned ACT_BYTES = (unsigned)S_ * D_ * 2;
constexpr unsigned WFF_BYTES = (unsigned)D_ * F_ * 2;
constexpr unsigned WQKV_BYTES = (unsigned)D_ * 3 * D_ * 2, WO_BYTES = (unsigned)D_ * D_ * 2;

constexpr unsigned WS_GEN = 0;
constexpr unsigned WS_FLAGS = 4096;
constexpr unsigned FL_A = 0, FL_B = 2, FL_C = 4, FL_E = 6, FL_S = 7;
constexpr unsigned WS_CS = 32768;
constexpr unsigned WS_STATS = 131072;
constexpr unsigned WS_O = 1u << 20;
constexpr unsigned WS_X1 = WS_O + 2 * ACT_BYTES;
constexpr unsigned WS_X2 = WS_X1 + 2 * ACT_BYTES;
constexpr unsigned WS_PART = WS_X2 + ACT_BYTES;
constexpr unsigned WS_END = WS_PART + 32 * ACT_BYTES;

DI rsrc_t mk_rsrc(const void* p, unsigned bytes) { return __builtin_amdgcn_make_buffer_rsrc((void*)p, 0, bytes, 0x00020000); }
typedef unsigned srd_t __attribute__((ext_vector_type(4)));
DI srd_t mk_srd(const void* p, unsigned bytes) {
    const u64 a = (u64)p;
    return (srd_t){(unsigned)a, (unsigned)(a >> 32) & 0xffffu, bytes, 0x00020000u};
}
template <int AUX = 0> DI void blds16(srd_t r, unsigned voff, unsigned soff, lptr l) {
    const unsigned la = (unsigned)(size_t)l;
    if (AUX) asm volatile("s_mov_b32 m0, %0\n\ts_nop 3\n\tbuffer_load_dwordx4 %1, %2, %3 offen sc1 lds" :: "s"(la), "v"(voff), "s"(r), "s"(soff) : "memory");
    else     asm volatile("s_mov_b32 m0, %0\n\ts_nop 3\n\tbuffer_load_dwordx4 %1, %2, %3 offen lds" :: "s"(la), "v"(voff), "s"(r), "s"(soff) : "memory");
}
template <int AUX> DI u32x4 ld16(rsrc_t r, unsigned voff, unsigned soff) { return __builtin_amdgcn_raw_buffer_load_b128(r, voff, soff, AUX); }
template <int AUX> DI u32x2 ld8(rsrc_t r, unsigned voff, unsigned soff) { return __builtin_amdgcn_raw_buffer_load_b64(r, voff, soff, AUX); }
DI void st16(rsrc_t r, unsigned voff, unsigned soff, u32x4 v) { __builtin_amdgcn_raw_buffer_store_b128(v, r, voff, soff, SC1); }
DI void st8(rsrc_t r, unsigned voff, unsigned soff, u32x2 v) { __builtin_amdgcn_raw_buffer_store_b64(v, r, voff, soff, SC1); }
DI bf16x8 lds_frag(lptr p) { return *(const LAS bf16x8*)p; }
DI bf16x8 lds_tr2(lptr p0, lptr p1) {
    bf16x4 lo = __builtin_amdgcn_ds_read_tr16_b64_v4bf16((LAS bf16x4*)p0);
    bf16x4 hi = __builtin_amdgcn_ds_read_tr16_b64_v4bf16((LAS bf16x4*)p1);
    return __builtin_shufflevector(lo, hi, 0, 1, 2, 3, 4, 5, 6, 7);
}
DI f32x4 mfma16(bf16x8 a, bf16x8 b, f32x4 c) { return __builtin_amdgcn_mfma_f32_16x16x32_bf16(a, b, c, 0, 0, 0); }
DI bf16x8 as_frag(u32x4 v) { return __builtin_bit_cast(bf16x8, v); }
DI unsigned pack2(float a, float b) {
    f32x2 f = {a, b};
    bf16x2 h = __builtin_convertvector(f, bf16x2);
    return __builtin_bit_cast(unsigned, h);
}
DI float bf_lo(unsigned u) { return __uint_as_float(u << 16); }
DI float bf_hi(unsigned u) { return __uint_as_float(u & 0xffff0000u); }
DI float relu(float x) { const int i = __float_as_int(x); return __int_as_float(i > 0 ? i : 0); }
DI float red_sum_g(float v) {
    auto a = __builtin_amdgcn_permlane16_swap(__float_as_uint(v), __float_as_uint(v), false, false);
    v = __uint_as_float(a[0]) + __uint_as_float(a[1]);
    auto b = __builtin_amdgcn_permlane32_swap(__float_as_uint(v), __float_as_uint(v), false, false);
    return __uint_as_float(b[0]) + __uint_as_float(b[1]);
}
DI float red_max_g(float v) {
    auto a = __builtin_amdgcn_permlane16_swap(__float_as_uint(v), __float_as_uint(v), false, false);
    v = fmaxf(__uint_as_float(a[0]), __uint_as_float(a[1]));
    auto b = __builtin_amdgcn_permlane32_swap(__float_as_uint(v), __float_as_uint(v), false, false);
    return fmaxf(__uint_as_float(b[0]), __uint_as_float(b[1]));
}
DI int opaque_lane() { int l; asm volatile("v_mbcnt_lo_u32_b32 %0, -1, 0\n\tv_mbcnt_hi_u32_b32 %0, -1, %0" : "=v"(l)); return l; }
DI int opaque_s(int v) { asm volatile("" : "+s"(v)); return v; }

DI void publish(char* ws, unsigned arr, unsigned idx, u64 tag, int w, int lane) {
    WAIT_V(0);
    BAR();
    if (w == 0 && lane == 0)
        __hip_atomic_store((GAS u64*)(ws + WS_FLAGS + arr * 2048 + idx * 8), tag, __ATOMIC_RELAXED, __HIP_MEMORY_SCOPE_AGENT);
}
DI void poll(char* ws, unsigned off, u64 tag) {
    GAS u64* p = (GAS u64*)(ws + WS_FLAGS + off);
    for (unsigned it = 0; it < (1u << 20); ++it) {
        const u64 v = __hip_atomic_load(p, __ATOMIC_RELAXED, __HIP_MEMORY_SCOPE_AGENT);
        if (__all(v == tag)) break;
        __builtin_amdgcn_s_sleep(1);
    }
    CFENCE();
}

constexpr int A_RING = 92160;
constexpr int A_KV = 0, A_KV_RS = 528;
constexpr int A_Q = 42240, A_Q_RS = 272;
constexpr int A_O = 59648, A_O_RS = 160;
constexpr int A_ST = 69888;
constexpr int B_W = 71680;
constexpr int B_RED = 0;

DI void stage_a(lptr sm, char* ws, const char* xin, const char* wqkv_, const char* wo_, const char* wg_, const char* wu_,
                const int l_, const int w_, const u64 tag) {
    const int lane = opaque_lane();
    const int bid = opaque_s(blockIdx.x), w = opaque_s(w_), l = opaque_s(l_);
    const int tt = (bid & 7) * 4 + ((bid >> 3) & 3), hg = bid >> 5;
    const int t = lane & 15, g = lane >> 4, q = (lane >> 2) & 3, pp = lane & 3;
    const srd_t rw = mk_srd(wqkv_, WQKV_BYTES), rx = mk_srd(xin, ACT_BYTES);
    const rsrc_t rws = mk_rsrc(ws, WS_END);

    unsigned woff[2];
    lptr ring = sm + A_RING + (w < 7 ? w : 0) * 10240;
#pragma unroll
    for (int i = 0; i < 2; ++i) {
        const int p = i * 16 + (lane >> 2);
        int c = 4 * w + ((lane & 3) ^ (((p >> 3) & 1) << 1));
        c = c > 26 ? 26 : c;
        const int mat = c / 9, cc = c - mat * 9;
        woff[i] = (unsigned)(p * 3456 + mat * 1152 + hg * 144 + cc * 16);
    }
    if (w < 7) {
#pragma unroll
        for (int s = 0; s < 4; ++s) {
            blds16(rw, woff[0], s * 32 * 3456, ring + s * 2048);
            blds16(rw, woff[1], s * 32 * 3456, ring + s * 2048 + 1024);
        }
    } else {
        if (lane < 8) {
#pragma unroll
            for (int kb = 0; kb < 9; ++kb) *(LAS u32x4*)(sm + kb * 10240 + lane * 16) = (u32x4){0x3f803f80u, 0x3f803f80u, 0x3f803f80u, 0x3f803f80u};
        }
        if (l > 0) { int i = 8 * tt - 4 + (lane < 12 ? lane : 11); i = i < 0 ? 0 : i; poll(ws, FL_E * 2048 + i * 8, tag); }
    }
    if (l > 0) BAR();
    STAMP(l * 32 + 1);
    {
        const unsigned xo = (unsigned)((lane >> 3) * 1152 + ((lane & 7) ^ (lane >> 3)) * 16);
#pragma unroll
        for (int i = 0; i < 12; ++i) {
            const int idx = i * 8 + w;
            if (idx < 90) {
                const int kb = idx / 10, rb = idx - kb * 10;
                int r0 = tt * 64 - 16 + rb * 8; r0 = r0 < 0 ? r0 + 16 : r0;
                if (rb == 0) { if (lane >= 8) blds16<SC1>(rx, xo, r0 * 1152 + kb * 128, sm + idx * 1024); }
                else blds16<SC1>(rx, xo, r0 * 1152 + kb * 128, sm + idx * 1024);
            }
        }
    }
    f32x4 acc[2][5];
#pragma unroll
    for (int j = 0; j < 2; ++j)
#pragma unroll
        for (int i = 0; i < 5; ++i) acc[j][i] = (f32x4){0.f, 0.f, 0.f, 0.f};
    lptr ta0 = sm + t * 128 + ((g ^ (t & 7)) * 16), ta1 = sm + t * 128 + (((4 + g) ^ (t & 7)) * 16);

    if (w < 7) {
        WAIT_V(0); WAIT_L0(); BAR();
        STAMP(l * 32 + 2);
        lptr wb0 = ring + (8 * g + q) * 64 + ((g & 1) * 32) + pp * 8;
        lptr wb1 = ring + (8 * g + q) * 64 + (((g & 1) ^ 1) * 32) + pp * 8;
#pragma unroll
        for (int s = 0; s < 18; ++s) {
            if (s >= 4) { if (s <= 14) { WAIT_V(6); } else if (s == 15) { WAIT_V(4); } else if (s == 16) { WAIT_V(2); } else { WAIT_V(0); } }
            const int slot = (s % 5) * 2048;
            bf16x8 wf0 = lds_tr2(wb0 + slot, wb0 + slot + 256);
            bf16x8 wf1 = lds_tr2(wb1 + slot, wb1 + slot + 256);
            bf16x8 tk[5];
            lptr ta = ((s & 1) ? ta1 : ta0) + (s >> 1) * 10240;
#pragma unroll
            for (int i = 0; i < 5; ++i) tk[i] = lds_frag(ta + i * 2048);
            if (s + 4 < 18) {
                const int ns = ((s + 4) % 5) * 2048;
                blds16(rw, woff[0], (s + 4) * 32 * 3456, ring + ns);
                blds16(rw, woff[1], (s + 4) * 32 * 3456, ring + ns + 1024);
            }
#pragma unroll
            for (int i = 0; i < 5; ++i) {
                acc[0][i] = mfma16(wf0, tk[i], acc[0][i]);
                acc[1][i] = mfma16(wf1, tk[i], acc[1][i]);
            }
        }
    } else {
        const bool do_cs = (l == 0) && (bid < 144);
        const rsrc_t rc = mk_rsrc(bid < 72 ? wg_ : wu_, do_cs ? WFF_BYTES : 0u);
        const unsigned co = (unsigned)((lane >> 3) * 9216 + (lane & 7) * 16), cb = (unsigned)((bid < 72 ? bid : bid - 72) * 128);
        u32x4 sel;
#pragma unroll
        for (int e = 0; e < 4; ++e) sel[e] = (t == 2 * e) ? 0x00003f80u : ((t == 2 * e + 1) ? 0x3f800000u : 0u);
        const bf16x8 ones = as_frag((u32x4){0x3f803f80u, 0x3f803f80u, 0x3f803f80u, 0x3f803f80u});
        f32x4 ca = {0.f, 0.f, 0.f, 0.f};
        WAIT_V(0); WAIT_L0(); BAR();
#pragma unroll
        for (int it = 0; it < 3; ++it) {
            u32x4 v[24];
            if (do_cs) {
#pragma unroll
                for (int j = 0; j < 24; ++j) v[j] = ld16<0>(rc, co, cb + (it * 24 + j) * 8 * 9216);
            }
#pragma unroll
            for (int s = it * 6; s < it * 6 + 6; ++s) {
                lptr ta = ((s & 1) ? ta1 : ta0) + (s >> 1) * 10240;
#pragma unroll
                for (int i = 0; i < 5; ++i) {
                    const bf16x8 tk = lds_frag(ta + i * 2048);
                    acc[0][i] = mfma16(ones, tk, acc[0][i]);
                    acc[1][i] = mfma16(tk, tk, acc[1][i]);
                }
            }
            if (do_cs) {
#pragma unroll
                for (int j = 0; j < 24; ++j) ca = mfma16(as_frag(sel), as_frag(v[j]), ca);
            }
        }
        if (do_cs) {
#pragma unroll
            for (int r = 0; r < 4; ++r) ca[r] += __shfl_xor(ca[r], 8);
            if (t < 8 && g < 2) st16(rws, (unsigned)(WS_CS + (bid * 64 + t * 8 + g * 4) * 4), 0, __builtin_bit_cast(u32x4, ca));
        }
    }
    WAIT_L0();
    BAR();
    STAMP(l * 32 + 3);
    {
        const srd_t ro = mk_srd(wo_, WO_BYTES);
#pragma unroll
        for (int i = 0; i < 12; ++i) {
            const int ins = i * 8 + w;
            if (ins < 90) {
                const int qq = ins * 64 + lane;
                const int p = qq / 10; int c = qq - p * 10; c = c > 8 ? 8 : c;
                const int r = p ^ (((p >> 3) & 1) << 2);
                blds16(ro, (unsigned)(r * 1152 + c * 16), hg * 144, sm + B_W + ins * 1024);
            }
        }
    }
    float csb[2][4];
    if (w == 7) {
#pragma unroll
        for (int i = 0; i < 5; ++i) {
            const float sq = (t & 2) ? ((t & 1) ? acc[1][i][3] : acc[1][i][2]) : ((t & 1) ? acc[1][i][1] : acc[1][i][0]);
            const float mean = acc[0][i][0] * (1.0f / D_);
            const float var = fmaxf(sq * (1.0f / D_) - mean * mean, 0.0f);
            const float rstd = 1.0f / sqrtf(var + LN_EPS);
            if (g == (t >> 2)) *(LAS f32x2*)(sm + A_ST + (16 * i + t) * 8) = (f32x2){mean * rstd, rstd};
        }
    }
#pragma unroll
    for (int j = 0; j < 2; ++j)
#pragma unroll
        for (int r = 0; r < 4; ++r) csb[j][r] = __shfl(acc[j][0][r], lane & 48);
    WAIT_L0();
    BAR();
    if (w < 7) {
        f32x2 stt[5];
#pragma unroll
        for (int i = 0; i < 5; ++i) stt[i] = *(const LAS f32x2*)(sm + A_ST + (16 * i + t) * 8);
#pragma unroll
        for (int j = 0; j < 2; ++j) {
#pragma unroll
            for (int hf = 0; hf < 2; ++hf) {
                const int c = 32 * w + 16 * j + 4 * g + 2 * hf;
                const int sl = c / 18, e = c - sl * 18;
                const bool isq = sl < 4;
                const int rs = isq ? A_Q_RS : A_KV_RS;
                lptr base = sm + (isq ? A_Q + sl * 64 - 16 * A_Q_RS : A_KV + (sl - 4) * 64) + e * 2 + t * rs;
#pragma unroll
                for (int i = 0; i < 5; ++i) {
                    const float a = acc[j][i][2 * hf] * stt[i][1] - stt[i][0] * csb[j][2 * hf];
                    const float b = acc[j][i][2 * hf + 1] * stt[i][1] - stt[i][0] * csb[j][2 * hf + 1];
                    if (c < 216 && (i > 0 || !isq)) *(LAS unsigned*)(base + 16 * i * rs) = pack2(a, b);
                }
            }
        }
    }
    WAIT_L0();
    BAR();
    STAMP(l * 32 + 4);
    {
        const int i = w >> 1;
        const int pos = tt * 64 + 16 * i + t;
        const unsigned m0 = (g < 3) ? 0xffffffffu : 0u, m1 = (g < 2) ? 0xffffffffu : 0u;
        bool ok[2][4];
#pragma unroll
        for (int a = 0; a < 2; ++a)
#pragma unroll
            for (int r = 0; r < 4; ++r) {
                const int dl = 16 + t - 16 * a - 4 * g - r;
                ok[a][r] = ((dl & 1) == 0) && dl >= 0 && dl <= 14 && dl <= pos;
            }
#pragma unroll
        for (int hh = 0; hh < 2; ++hh) {
            const int h = 2 * (w & 1) + hh;
            u32x4 qf = *(const LAS u32x4*)(sm + A_Q + (16 * i + t) * A_Q_RS + h * 64 + g * 16);
            qf[0] &= m0; qf[1] &= m1; qf[2] &= m1; qf[3] &= m1;
            f32x4 sc[2];
#pragma unroll
            for (int a = 0; a < 2; ++a) {
                u32x4 kf = *(const LAS u32x4*)(sm + A_KV + (16 * i + 16 * a + t) * A_KV_RS + h * 64 + g * 16);
                kf[0] &= m0; kf[1] &= m1; kf[2] &= m1; kf[3] &= m1;
                sc[a] = mfma16(as_frag(kf), as_frag(qf), (f32x4){0.f, 0.f, 0.f, 0.f});
            }
            lptr vb = sm + A_KV + (16 * i + 4 * g + q) * A_KV_RS + 256 + h * 64 + pp * 8;
            const bf16x8 vf0 = lds_tr2(vb, vb + 16 * A_KV_RS), vf1 = lds_tr2(vb + 32, vb + 32 + 16 * A_KV_RS);
            float mx = -3.0e38f;
#pragma unroll
            for (int a = 0; a < 2; ++a)
#pragma unroll
                for (int r = 0; r < 4; ++r) { sc[a][r] = ok[a][r] ? sc[a][r] : -3.0e38f; mx = fmaxf(mx, sc[a][r]); }
            mx = red_max_g(mx);
            constexpr float kScale = 0.23570226039551584f * 1.4426950408889634f;
            float sum = 0.f;
#pragma unroll
            for (int a = 0; a < 2; ++a)
#pragma unroll
                for (int r = 0; r < 4; ++r) {
                    const float e = __builtin_amdgcn_exp2f((sc[a][r] - mx) * kScale);
                    sc[a][r] = ok[a][r] ? e : 0.f; sum += sc[a][r];
                }
            sum = red_sum_g(sum);
            const float inv = __builtin_amdgcn_rcpf(sum);
            u32x4 pf;
            pf[0] = pack2(sc[0][0] * inv, sc[0][1] * inv); pf[1] = pack2(sc[0][2] * inv, sc[0][3] * inv);
            pf[2] = pack2(sc[1][0] * inv, sc[1][1] * inv); pf[3] = pack2(sc[1][2] * inv, sc[1][3] * inv);
            const f32x4 o0 = mfma16(vf0, as_frag(pf), (f32x4){0.f, 0.f, 0.f, 0.f});
            const f32x4 o1 = mfma16(vf1, as_frag(pf), (f32x4){0.f, 0.f, 0.f, 0.f});
            lptr op = sm + A_O + (16 * i + t) * A_O_RS + h * 36;
            *(LAS unsigned*)(op + g * 8) = pack2(o0[0], o0[1]);
            *(LAS unsigned*)(op + g * 8 + 4) = pack2(o0[2], o0[3]);
            if (g == 0) *(LAS unsigned*)(op + 32) = pack2(o1[0], o1[1]);
        }
    }
    WAIT_L0();
    BAR();
    STAMP(l * 32 + 5);
#pragma unroll
    for (int k = 0; k < 2; ++k) {
        if (k == 0 || w == 0) {
            const int c = k * 8 + w;
            const u32x4 v = *(const LAS u32x4*)(sm + A_O + lane * A_O_RS + c * 16);
            const int cc = hg * 9 + c;
            st16(rws, (unsigned)((lane >> 4) * 1024 + (lane & 15) * 16),
                 WS_O + l * ACT_BYTES + ((cc >> 2) * 128 + tt * 4) * 1024 + (cc & 3) * 256, v);
        }
    }
    publish(ws, FL_A + l, tt * 8 + hg, tag, w, lane);
    if (l == 0 && w == 1 && lane == 0)
        __hip_atomic_store((GAS u64*)(ws + WS_FLAGS + FL_S * 2048 + bid * 8), tag, __ATOMIC_RELAXED, __HIP_MEMORY_SCOPE_AGENT);
    STAMP(l * 32 + 6);
}

DI void stage_b(lptr sm, char* ws, const char* xin, const int l_, const int w_, const u64 tag) {
    const int lane = opaque_lane();
    const int bid = opaque_s(blockIdx.x), w = opaque_s(w_), l = opaque_s(l_);
    const int tt = (bid & 7) * 4 + ((bid >> 3) & 3), cb = bid >> 5;
    const int t = lane & 15, g = lane >> 4;
    const int ti = w & 3, kh = w >> 2;
    const int token = tt * 64 + ti * 16 + t;
    const rsrc_t rws = mk_rsrc(ws, WS_END), rx = mk_rsrc(xin, ACT_BYTES);

    u32x2 res[5];
    if (w == 7) poll(ws, (FL_A + l) * 2048 + (tt * 8 + (lane & 7)) * 8, tag);
    if (kh == 0) {
        const unsigned o = (unsigned)(token * 1152 + g * 8);
#pragma unroll
        for (int j = 0; j < 4; ++j) res[j] = ld8<SC1>(rx, o + j * 32, cb * 144);
        res[4] = ld8<SC1>(rx, o + ((g < 2) ? 128 : 0), cb * 144);
    }
    BAR();
    STAMP(l * 32 + 7);
    bf16x8 of[9];
#pragma unroll
    for (int s = 0; s < 9; ++s) of[s] = as_frag(ld16<SC1>(rws, lane * 16, WS_O + l * ACT_BYTES + ((kh * 9 + s) * 128 + tt * 4 + ti) * 1024));
    CFENCE(); SCHED();
    f32x4 acc[5];
#pragma unroll
    for (int j = 0; j < 5; ++j) acc[j] = (f32x4){0.f, 0.f, 0.f, 0.f};
    {
        const int q = (lane >> 2) & 3, pp = lane & 3;
        lptr b0 = sm + B_W + kh * 46080 + (8 * g + 4 * (g & 1) + q) * 160 + pp * 8;
        lptr b1 = sm + B_W + kh * 46080 + (8 * g + 4 * (1 - (g & 1)) + q) * 160 + pp * 8;
#pragma unroll
        for (int s = 0; s < 9; ++s)
#pragma unroll
            for (int j = 0; j < 5; ++j) {
                const bf16x8 wf = lds_tr2(b0 + s * 5120 + j * 32, b1 + s * 5120 + j * 32);
                acc[j] = mfma16(wf, of[s], acc[j]);
            }
    }
    if (kh == 1) {
#pragma unroll
        for (int j = 0; j < 5; ++j) *(LAS f32x4*)(sm + B_RED + (ti * 5 + j) * 1024 + lane * 16) = acc[j];
    }
    WAIT_L0();
    BAR();
    STAMP(l * 32 + 8);
    if (kh == 0) {
        float v[5][4];
        float s = 0.f;
#pragma unroll
        for (int j = 0; j < 5; ++j) {
            const f32x4 o = *(const LAS f32x4*)(sm + B_RED + (ti * 5 + j) * 1024 + lane * 16);
            const f32x4 a = acc[j] + o;
            u32x2 pk;
            pk[0] = pack2(a[0] + bf_lo(res[j][0]), a[1] + bf_hi(res[j][0]));
            pk[1] = pack2(a[2] + bf_lo(res[j][1]), a[3] + bf_hi(res[j][1]));
            const bool ok = (j < 4) || (g < 2);
            v[j][0] = bf_lo(pk[0]); v[j][1] = bf_hi(pk[0]); v[j][2] = bf_lo(pk[1]); v[j][3] = bf_hi(pk[1]);
            if (ok) {
                s += (v[j][0] + v[j][1]) + (v[j][2] + v[j][3]);
                const int d = cb * 72 + 16 * j + 4 * g;
                st8(rws, (unsigned)((d >> 5) * 131072 + ((d >> 3) & 3) * 256 + t * 16 + (d & 7) * 2), WS_X1 + l * ACT_BYTES + (tt * 4 + ti) * 1024, pk);
            }
        }
        s = red_sum_g(s);
        const float mean = s * (1.0f / 72.0f);
        float q = 0.f;
#pragma unroll
        for (int j = 0; j < 5; ++j) {
            const bool ok = (j < 4) || (g < 2);
#pragma unroll
            for (int r = 0; r < 4; ++r) { const float d = v[j][r] - mean; q += ok ? d * d : 0.f; }
        }
        q = red_sum_g(q);
        if (g == 0) st8(rws, (unsigned)(token * 64), WS_STATS + l * 131072 + cb * 8, (u32x2){__float_as_uint(mean), __float_as_uint(q)});
    }
    WAIT_L0();
    publish(ws, FL_B + l, tt * 8 + cb, tag, w, lane);
    STAMP(l * 32 + 9);
}

constexpr int C_BUF = 45056;
constexpr int C_H = 90112;

DI void stage_c(lptr sm, char* ws, const char* wg_, const char* wu_, const char* wd_, const int l_, const int w_, const u64 tag) {
    const int lane = opaque_lane();
    const int bid = opaque_s(blockIdx.x), w = opaque_s(w_), l = opaque_s(l_);
    const int fs = 2 * (bid & 7) + ((bid >> 3) & 1), T = bid >> 4;
    const int m = w >> 2, n = w & 3;
    const int t = lane & 15, g = lane >> 4;
    const int q = (lane >> 2) & 3, pp = lane & 3;
    const rsrc_t rws = mk_rsrc(ws, WS_END);
    const srd_t rx = mk_srd(ws, WS_END), rg = mk_srd(wg_, WFF_BYTES), ru = mk_srd(wu_, WFF_BYTES), rd = mk_srd(wd_, WFF_BYTES);
    const srd_t rs = mk_srd((n & 1) ? wu_ : wg_, WFF_BYTES);
    RDECL(s0); RDECL(s1); RDECL(s2); RDECL(s3); RDECL(s4); RDECL(s5);

    unsigned so[3];
    const int i0 = (1 - m) * 8 + 2 * n;
#pragma unroll
    for (int k = 0; k < 3; ++k) {
        const int ins = (k < 2) ? i0 + k : 16 + (n >> 1);
        const int qq = ins * 64 + lane;
        const int p = qq / 36, c = qq - p * 36;
        so[k] = (unsigned)(p * 9216 + (c ^ (((p >> 3) & 1) << 1)) * 16);
    }
    const int xdst = (n & 1) * 18432 + (16 + (n >> 1)) * 1024;
    const unsigned l16 = lane * 16;
    const unsigned sd = (unsigned)((lane >> 3) * 1152 + ((lane & 7) ^ (((lane >> 4) & 3) << 1)) * 16);
    const unsigned tkb = WS_X1 + l * ACT_BYTES + (T * 8 + w) * 1024;
    const int d0 = m ? 4 * n : 16 + 5 * n;

#define C_W(k, s, buf) do { const unsigned ko_ = (unsigned)(s) * (32 * 9216) + fs * 576; lptr b_ = sm + (buf); \
        if ((k) == 0) blds16(rg, so[0], ko_, b_ + i0 * 1024); \
        if ((k) == 1) blds16(ru, so[0], ko_, b_ + 18432 + i0 * 1024); \
        if ((k) == 2) blds16(rg, so[1], ko_, b_ + (i0 + 1) * 1024); \
        if ((k) == 3) blds16(ru, so[1], ko_, b_ + 18432 + (i0 + 1) * 1024); \
        if ((k) == 4) { if (m == 0) blds16(rs, so[2], ko_, b_ + xdst); } \
        if ((k) == 5) blds16<SC1>(rx, l16, tkb + (unsigned)(s) * 131072, b_ + 36864 + w * 1024); \
        CFENCE(); } while (0)
#define C_D(k, b, buf) do { \
        if ((k) < 4 || ((k) == 4 && m == 0)) blds16(rd, sd, (unsigned)((fs * 288 + (d0 + (k)) * 8) * 1152 + (b) * 128), sm + (buf) + (d0 + (k)) * 1024); \
        CFENCE(); } while (0)
#define C_DALL(b, buf) do { C_D(0, b, buf); C_D(1, b, buf); C_D(2, b, buf); C_D(3, b, buf); C_D(4, b, buf); } while (0)

    if (w == 7) {
        unsigned off = (FL_B + l) * 2048 + (T * 16 + (lane & 15)) * 8;
        if (l == 0 && lane >= 16 && lane < 28) {
            const int k = lane - 16, kk = k < 6 ? k : k - 6;
            int blk = (fs * 288) / 64 + kk; const int last = (fs * 288 + 287) / 64; blk = blk > last ? last : blk;
            off = FL_S * 2048 + (blk + (k < 6 ? 0 : 72)) * 8;
        }
        poll(ws, off, tag);
    }
#pragma unroll
    for (int k = 0; k < 5; ++k) { C_W(k, 0, 0); }
#pragma unroll
    for (int k = 0; k < 5; ++k) { C_W(k, 1, C_BUF); }
    BAR();
    STAMP(l * 32 + 10);
    C_W(5, 0, 0);
    C_W(5, 1, C_BUF);
    f32x4 acc[9][4];
    {
        f32x4 st[4], csv[9];
#pragma unroll
        for (int i = 0; i < 4; ++i)
            st[i] = __builtin_bit_cast(f32x4, ld16<SC1>(rws, (unsigned)(t * 64 + g * 16), WS_STATS + l * 131072 + (T * 128 + m * 64 + i * 16) * 64));
#pragma unroll
        for (int j = 0; j < 4; ++j) {
            csv[j] = __builtin_bit_cast(f32x4, ld16<SC1>(rws, (unsigned)(g * 16), WS_CS + (fs * 288 + (4 * n + j) * 16) * 4));
            csv[4 + j] = __builtin_bit_cast(f32x4, ld16<SC1>(rws, (unsigned)(g * 16), WS_CS + (F_ + fs * 288 + (4 * n + j) * 16) * 4));
        }
        csv[8] = __builtin_bit_cast(f32x4, ld16<SC1>(rws, (unsigned)((g >> 1) * F_ * 4 + (g & 1) * 16), WS_CS + (fs * 288 + 256 + 8 * n) * 4));
#pragma unroll
        for (int i = 0; i < 4; ++i) {
            const float nmean = red_sum_g(st[i][0] + st[i][2]) * -0.125f;
#pragma unroll
            for (int j = 0; j < 9; ++j) acc[j][i] = csv[j] * nmean;
        }
    }

    const int rowoff = (8 * g + q) * 576 + pp * 8;
    const int be = rowoff + 128 * n + 32 * (g & 1);
    const int bo = rowoff + 128 * n - 32 * (g & 1);
    const int bm = (8 * g + q) * 576 + (((32 + n) ^ (2 * (g & 1))) * 16) + 8 * (pp & 1) + (pp >> 1) * 18432;
    const int bt = 36864 + m * 4096 + lane * 16;

#define C_STEP1(cur, ST, a0, a1, NX, NY) do { \
        bf16x8 tk[4], wf[9]; \
        lptr b = sm + (cur); \
        ST(0, a0, a1); \
        _Pragma("unroll") for (int i = 0; i < 4; ++i) tk[i] = lds_frag(b + bt + i * 1024); \
        CFENCE(); \
        _Pragma("unroll") for (int j = 0; j < 4; ++j) { \
            ST(j + 1, a0, a1); \
            lptr p = b + ((j & 1) ? bo : be) + 32 * j; \
            wf[j] = lds_tr2(p, p + 2304); \
            wf[4 + j] = lds_tr2(p + 18432, p + 18432 + 2304); \
            CFENCE(); } \
        ST(5, a0, a1); \
        wf[8] = lds_tr2(b + bm, b + bm + 2304); \
        if (m == 1) WAIT_V(NY); \
        WAIT_L0(); SCHED(); BAR(); \
        __builtin_amdgcn_s_setprio(1); \
        _Pragma("unroll") for (int j = 0; j < 9; ++j) \
            _Pragma("unroll") for (int i = 0; i < 4; ++i) acc[j][i] = mfma16(wf[j], tk[i], acc[j][i]); \
        __builtin_amdgcn_s_setprio(0); \
        SCHED(); \
        if (m == 0) WAIT_V(NX); \
        BAR(); \
    } while (0)

    SCHED();
    WAIT_V(0);
    BAR();
    RSTAMP(s0);
    if (m == 1) BAR();
    int cur = 0;
    for (int s = 0; s < 16; ++s) {
        const int nxt2 = (cur == 0) ? 2 * C_BUF : cur - C_BUF;
        C_STEP1(cur, C_W, s + 2, nxt2, 6, 5);
        cur = (cur == 2 * C_BUF) ? 0 : cur + C_BUF;
    }
    RSTAMP(s1);
    C_STEP1(C_BUF, C_D, 0, 0, 5, 4);
    C_STEP1(2 * C_BUF, C_D, 1, C_BUF, 5, 4);
    RSTAMP(s2);

    {
        lptr hb = sm + C_H + m * 36864;
#pragma unroll
        for (int i = 0; i < 4; ++i)
#pragma unroll
            for (int a = 0; a < 2; ++a) {
                u32x4 o;
#pragma unroll
                for (int k = 0; k < 2; ++k) {
                    const f32x4 gt = acc[2 * a + k][i], up = acc[4 + 2 * a + k][i];
                    o[2 * k] = pack2(relu(gt[0]) * up[0], relu(gt[1]) * up[1]);
                    o[2 * k + 1] = pack2(relu(gt[2]) * up[2], relu(gt[3]) * up[3]);
                }
                *(LAS u32x4*)(hb + (i * 9 + 2 * n + a) * 1024 + lane * 16) = o;
            }
#pragma unroll
        for (int i = 0; i < 4; i += 2) {
            float hv[4];
#pragma unroll
            for (int r = 0; r < 4; ++r) {
                auto x = __builtin_amdgcn_permlane32_swap(__float_as_uint(acc[8][i][r]), __float_as_uint(acc[8][i + 1][r]), false, false);
                hv[r] = relu(__uint_as_float(x[0])) * __uint_as_float(x[1]);
            }
            u32x2 o; o[0] = pack2(hv[0], hv[1]); o[1] = pack2(hv[2], hv[3]);
            *(LAS u32x2*)(hb + ((i + (g >> 1)) * 9 + 8) * 1024 + (2 * (n & 1) + (g & 1)) * 256 + t * 16 + (n >> 1) * 8) = o;
        }
    }
    WAIT_V(0);
    WAIT_L0();
    SCHED();
    BAR();
    RSTAMP(s3);
    bf16x8 hf[4][9];
    const int bd = (4 * g + q) * 128 + ((n ^ ((2 * g + (q >> 1)) & 3)) * 32) + pp * 8;
    const unsigned po = (unsigned)((t >> 2) * 9216 + g * 64 + (t & 3) * 16);
    const unsigned pb = WS_PART + (l * 16 + fs) * ACT_BYTES + (T * 2 + m) * 73728 + n * 256;
    f32x4 a2[4];
#define C_SLOT(b) (((b) & 2) ? C_H + ((b) & 1) * 36864 : ((b) & 1) * C_BUF)
#define C_STORE(b) do { \
        _Pragma("unroll") for (int ip = 0; ip < 2; ++ip) { \
            u32x4 o; \
            o[0] = pack2(a2[2 * ip][0], a2[2 * ip][1]); o[1] = pack2(a2[2 * ip][2], a2[2 * ip][3]); \
            o[2] = pack2(a2[2 * ip + 1][0], a2[2 * ip + 1][1]); o[3] = pack2(a2[2 * ip + 1][2], a2[2 * ip + 1][3]); \
            st16(rws, po, pb + ip * 36864 + (b) * 1024, o); } \
        CFENCE(); \
    } while (0)
#pragma unroll
    for (int b = 0; b < 9; ++b) {
        bf16x8 wf[9];
        lptr pw = sm + C_SLOT(b) + bd;
        if (b == 0) {
            if (m == 1) C_DALL(2, C_SLOT(2));
#pragma unroll
            for (int s = 0; s < 9; ++s) wf[s] = lds_tr2(pw + s * 4096, pw + s * 4096 + 2048);
            lptr hb = sm + C_H + m * 36864 + lane * 16;
#pragma unroll
            for (int s = 0; s < 9; ++s)
#pragma unroll
                for (int i = 0; i < 4; ++i) hf[i][s] = lds_frag(hb + (i * 9 + s) * 1024);
        } else {
            if (b == 1) C_DALL(3, C_SLOT(3));
#pragma unroll
            for (int s = 0; s < 9; ++s) {
                if (b + 3 < 9 && (s & 1) == 0) C_D(s >> 1, b + 3, C_SLOT(b + 3));
                wf[s] = lds_tr2(pw + s * 4096, pw + s * 4096 + 2048);
                CFENCE();
            }
            C_STORE(b - 1);
        }
        if (m == 1) {
            if (b == 1) WAIT_V(10); else if (b == 2) WAIT_V(12); else if (b >= 3 && b <= 5) WAIT_V(14); else if (b == 6) WAIT_V(10); else if (b == 7) WAIT_V(6);
        }
        WAIT_L0();
        SCHED();
        BAR();
        if (b == 0) { if (m == 0) C_DALL(2, C_SLOT(2)); }
        __builtin_amdgcn_s_setprio(1);
#pragma unroll
        for (int i = 0; i < 4; ++i) a2[i] = mfma16(wf[0], hf[i][0], (f32x4){0.f, 0.f, 0.f, 0.f});
#pragma unroll
        for (int s = 1; s < 9; ++s)
#pragma unroll
            for (int i = 0; i < 4; ++i) a2[i] = mfma16(wf[s], hf[i][s], a2[i]);
        __builtin_amdgcn_s_setprio(0);
        SCHED();
        if (m == 0) {
            if (b == 1) WAIT_V(12); else if (b == 2) WAIT_V(14); else if (b >= 3 && b <= 5) WAIT_V(16); else if (b == 6) WAIT_V(11); else if (b == 7) WAIT_V(6);
        }
        if (b < 8 || m == 0) BAR();
        if (b == 0) RSTAMP(s4);
    }
    RSTAMP(s5);
    C_STORE(8);
    publish(ws, FL_C + l, T * 16 + fs, tag, w, lane);
    RFLUSH(s0, l * 32 + 11); RFLUSH(s1, l * 32 + 12); RFLUSH(s2, l * 32 + 13); RFLUSH(s3, l * 32 + 14); RFLUSH(s4, l * 32 + 15); RFLUSH(s5, l * 32 + 16);
    STAMP(l * 32 + 17);
#undef C_W
#undef C_D
#undef C_DALL
#undef C_STEP1
#undef C_SLOT
#undef C_STORE
}

DI void e_piece(rsrc_t rws, rsrc_t ro, const int l, const int R, const int pc, const float rsa, const float rsb) {
    u32x4 v[16];
#pragma unroll
    for (int f = 0; f < 16; ++f) v[f] = ld16<SC1>(rws, (unsigned)(pc * 16), WS_PART + (l * 16 + f) * ACT_BYTES + R * 9216);
    const int d0 = (pc >> 2) * 4;
    const int ta = (R >> 2) * 32 + (R & 3) * 4 + (pc & 3);
    const unsigned xo = (unsigned)((d0 >> 5) * 131072 + (ta >> 4) * 1024 + ((d0 >> 3) & 3) * 256 + (ta & 15) * 16 + (d0 & 7) * 2);
    const u32x2 ra = ld8<SC1>(rws, xo, WS_X1 + l * ACT_BYTES), rb = ld8<SC1>(rws, xo + 1024, WS_X1 + l * ACT_BYTES);
    float a[8];
#pragma unroll
    for (int e = 0; e < 8; ++e) a[e] = 0.f;
#pragma unroll
    for (int f = 0; f < 16; ++f)
#pragma unroll
        for (int e = 0; e < 4; ++e) { a[2 * e] += bf_lo(v[f][e]); a[2 * e + 1] += bf_hi(v[f][e]); }
    u32x2 oa, ob;
    oa[0] = pack2(a[0] * rsa + bf_lo(ra[0]), a[1] * rsa + bf_hi(ra[0])); oa[1] = pack2(a[2] * rsa + bf_lo(ra[1]), a[3] * rsa + bf_hi(ra[1]));
    ob[0] = pack2(a[4] * rsb + bf_lo(rb[0]), a[5] * rsb + bf_hi(rb[0])); ob[1] = pack2(a[6] * rsb + bf_lo(rb[1]), a[7] * rsb + bf_hi(rb[1]));
    const unsigned oo = (unsigned)(ta * 1152 + d0 * 2);
    st8(ro, oo, 0, oa);
    st8(ro, oo + 16 * 1152, 0, ob);
}

DI void stage_e(char* ws, char* xout, const int l_, const int w_, const u64 tag) {
    const int lane = opaque_lane();
    const int bid = opaque_s(blockIdx.x), w = opaque_s(w_), l = opaque_s(l_);
    const int fs = 2 * (bid & 7) + ((bid >> 3) & 1), T = bid >> 4;
    const int R = T * 16 + fs;
    const rsrc_t rws = mk_rsrc(ws, WS_END), ro = mk_rsrc(xout, ACT_BYTES);
    float rsa, rsb;
    {
        const int tok = (R >> 2) * 32 + (R & 3) * 4 + (lane & 3) + (lane >> 5) * 16;
        const u32x2 sv = ld8<SC1>(rws, (unsigned)(tok * 64 + ((lane >> 2) & 7) * 8), WS_STATS + l * 131072);
        const float mj = __uint_as_float(sv[0]);
        float ms = mj; ms += __shfl_xor(ms, 4); ms += __shfl_xor(ms, 8); ms += __shfl_xor(ms, 16);
        const float dm = mj - ms * 0.125f;
        float m2 = __uint_as_float(sv[1]) + 72.0f * dm * dm;
        m2 += __shfl_xor(m2, 4); m2 += __shfl_xor(m2, 8); m2 += __shfl_xor(m2, 16);
        const float rs = 1.0f / (m2 * (1.0f / D_) + LN_EPS);
        auto x = __builtin_amdgcn_permlane32_swap(__float_as_uint(rs), __float_as_uint(rs), false, false);
        rsa = __uint_as_float(x[0]); rsb = __uint_as_float(x[1]);
    }
    if (w == 7) poll(ws, (FL_C + l) * 2048 + (T * 16 + (lane & 15)) * 8, tag);
    BAR();
    STAMP(l * 32 + 18);
    e_piece(rws, ro, l, R, w * 64 + lane, rsa, rsb);
    if (w == 7) e_piece(rws, ro, l, R, 512 + lane, rsa, rsb);
    if (l == 0) publish(ws, FL_E, R, tag, w, lane);
    STAMP(l * 32 + 19);
}

__global__ __launch_bounds__(512, 2) void fused_layers(const char* __restrict__ x0, const char* __restrict__ wqkv, const char* __restrict__ wo,
                                                       const char* __restrict__ wg, const char* __restrict__ wu, const char* __restrict__ wd,
                                                       char* __restrict__ out, char* __restrict__ ws) {
    extern __shared__ __attribute__((aligned(16))) char smem_[];
    lptr sm = (lptr)smem_;
    const int w = __builtin_amdgcn_readfirstlane(threadIdx.x >> 6);
    STAMP(0);
    u64 gen = __hip_atomic_load((GAS u64*)(ws + WS_GEN), __ATOMIC_RELAXED, __HIP_MEMORY_SCOPE_AGENT);
    const u64 tag = (((u64)__builtin_amdgcn_readfirstlane((unsigned)(gen >> 32)) << 32) | (unsigned)__builtin_amdgcn_readfirstlane((unsigned)gen)) + 0x9E3779B97F4A7C15ull;
    for (int l = 0; l < 2; ++l) {
        const char* xin = l == 0 ? x0 : ws + WS_X2;
        char* xout = l == 0 ? ws + WS_X2 : out;
        stage_a(sm, ws, xin, wqkv, wo, wg, wu, l, w, tag);
        stage_b(sm, ws, xin, l, w, tag);
        stage_c(sm, ws, wg, wu, wd, l, w, tag);
        stage_e(ws, xout, l, w, tag);
    }
    if (blockIdx.x == 0 && w == 0) {
        const int lane = opaque_lane();
#pragma unroll
        for (int k = 0; k < 4; ++k) poll(ws, FL_A * 2048 + (k * 64 + lane) * 8, tag);
        if (lane == 0) __hip_atomic_store((GAS u64*)(ws + WS_GEN), tag, __ATOMIC_RELAXED, __HIP_MEMORY_SCOPE_AGENT);
    }
}

extern "C" void kernel_launch(void* const* d_in, const int* in_sizes, int n_in, void* d_out, int out_size, void* d_ws, size_t ws_size,
                              hipStream_t stream) {
    (void)in_sizes; (void)n_in; (void)out_size; (void)ws_size;
    (void)hipFuncSetAttribute((const void*)fused_layers, hipFuncAttributeMaxDynamicSharedMemorySize, 163840);
    fused_layers<<<256, 512, 163840, stream>>>((const char*)d_in[0], (const char*)d_in[1], (const char*)d_in[2], (const char*)d_in[4],
                                               (const char*)d_in[5], (const char*)d_in[6], (char*)d_out, (char*)d_ws);
}
```

```cpp
#define m4.cpp ../hip_kernels/v4b.cpp NO_WARMQ WARM1 1 WARM2
#include <hip/hip_runtime.h>
#include <hip/hip_bf16.h>

#define LAS __attribute__((address_space(3)))
#define GAS __attribute__((address_space(1)))
typedef __bf16 bf16x8 __attribute__((ext_vector_type(8)));
typedef __bf16 bf16x4 __attribute__((ext_vector_type(4)));
typedef __bf16 bf16x2 __attribute__((ext_vector_type(2)));
typedef float f32x4 __attribute__((ext_vector_type(4)));
typedef float f32x2 __attribute__((ext_vector_type(2)));
typedef unsigned u32x4 __attribute__((ext_vector_type(4)));
typedef unsigned u32x2 __attribute__((ext_vector_type(2)));
typedef unsigned long long u64;
typedef LAS char* lptr;
typedef __amdgpu_buffer_rsrc_t rsrc_t;

#define DI __device__ __forceinline__
#define WAIT_V(n) asm volatile("s_waitcnt vmcnt(" #n ")" ::: "memory")
#define WAIT_L0() asm volatile("s_waitcnt lgkmcnt(0)" ::: "memory")
#define CFENCE() asm volatile("" ::: "memory")
#define BAR() do { CFENCE(); __builtin_amdgcn_s_barrier(); CFENCE(); } while (0)
#define SCHED() __builtin_amdgcn_sched_barrier(0)
#define SC1 16

constexpr int S_ = 2048, D_ = 576, F_ = 4608;
constexpr float LN_EPS = 1e-5f;
constexpr unsigned ACT_BYTES = (unsigned)S_ * D_ * 2;
constexpr unsigned WFF_BYTES = (unsigned)D_ * F_ * 2;
constexpr unsigned WQKV_BYTES = (unsigned)D_ * 3 * D_ * 2, WO_BYTES = (unsigned)D_ * D_ * 2;

constexpr unsigned WS_GEN = 0;
constexpr unsigned WS_FLAGS = 4096;
constexpr unsigned FL_A = 0, FL_B = 2, FL_C = 4, FL_E = 6, FL_S = 7;
constexpr unsigned WS_MEAN = 65536;
constexpr unsigned WS_STATS = 131072;
constexpr unsigned WS_WOF = 1u << 19;
constexpr unsigned WS_WQF = WS_WOF + 8 * 92160;
constexpr unsigned WS_O = WS_WQF + 8 * 258048;
constexpr unsigned WS_X1 = WS_O + 2 * ACT_BYTES;
constexpr unsigned WS_X2 = WS_X1 + 2 * ACT_BYTES;
constexpr unsigned WS_PART = WS_X2 + ACT_BYTES;
constexpr unsigned WS_END = WS_PART + 32 * ACT_BYTES;

DI rsrc_t mk_rsrc(const void* p, unsigned bytes) { return __builtin_amdgcn_make_buffer_rsrc((void*)p, 0, bytes, 0x00020000); }
typedef unsigned srd_t __attribute__((ext_vector_type(4)));
DI srd_t mk_srd(const void* p, unsigned bytes) {
    const u64 a = (u64)p;
    return (srd_t){(unsigned)a, (unsigned)(a >> 32) & 0xffffu, bytes, 0x00020000u};
}
template <int AUX = 0> DI void dma(srd_t r, unsigned voff, unsigned sb, int si, unsigned lb, int li) {
    unsigned tmp;
    if (AUX) asm volatile("s_add_i32 m0, %3, %5\n\ts_add_i32 %0, %4, %6\n\tbuffer_load_dwordx4 %1, %2, %0 offen sc1 lds"
                          : "=&s"(tmp) : "v"(voff), "s"(r), "s"(lb), "s"(sb), "i"(li), "i"(si) : "memory", "scc");
    else     asm volatile("s_add_i32 m0, %3, %5\n\ts_add_i32 %0, %4, %6\n\tbuffer_load_dwordx4 %1, %2, %0 offen lds"
                          : "=&s"(tmp) : "v"(voff), "s"(r), "s"(lb), "s"(sb), "i"(li), "i"(si) : "memory", "scc");
}
DI void dma_masked(srd_t r, unsigned voff, unsigned sb, int si, unsigned lb, int li, u64 mask) {
    unsigned tmp;
    asm volatile("s_add_i32 m0, %3, %5\n\ts_add_i32 %0, %4, %6\n\ts_mov_b64 exec, %7\n\tbuffer_load_dwordx4 %1, %2, %0 offen sc1 lds\n\ts_mov_b64 exec, -1"
                 : "=&s"(tmp) : "v"(voff), "s"(r), "s"(lb), "s"(sb), "i"(li), "i"(si), "s"(mask) : "memory", "scc");
}
DI void warm(srd_t r, unsigned voff, unsigned soff, unsigned lb) {
    asm volatile("s_mov_b32 m0, %0\n\ts_nop 0\n\tbuffer_load_dword %1, %2, %3 offen sc1 lds" :: "s"(lb), "v"(voff), "s"(r), "s"(soff) : "memory");
}
template <int AUX> DI u32x4 ld16(rsrc_t r, unsigned voff, unsigned soff) { return __builtin_amdgcn_raw_buffer_load_b128(r, voff, soff, AUX); }
template <int AUX> DI u32x2 ld8(rsrc_t r, unsigned voff, unsigned soff) { return __builtin_amdgcn_raw_buffer_load_b64(r, voff, soff, AUX); }
DI void st16(rsrc_t r, unsigned voff, unsigned soff, u32x4 v) {
    __builtin_amdgcn_raw_buffer_store_b128(v, r, voff, soff, SC1);
    asm volatile("s_nop 1" :: "v"(v) : "memory");
}
DI void st4(rsrc_t r, unsigned voff, unsigned soff, unsigned v) { __builtin_amdgcn_raw_buffer_store_b32(v, r, voff, soff, SC1); }
template <int AUX> DI unsigned ld4(rsrc_t r, unsigned voff, unsigned soff) { return __builtin_amdgcn_raw_buffer_load_b32(r, voff, soff, AUX); }
DI void st8(rsrc_t r, unsigned voff, unsigned soff, u32x2 v) { __builtin_amdgcn_raw_buffer_store_b64(v, r, voff, soff, SC1); }
DI bf16x8 lds_frag(lptr p) { return *(const LAS bf16x8*)p; }
DI bf16x8 lds_tr2(lptr p0, lptr p1) {
    bf16x4 lo = __builtin_amdgcn_ds_read_tr16_b64_v4bf16((LAS bf16x4*)p0);
    bf16x4 hi = __builtin_amdgcn_ds_read_tr16_b64_v4bf16((LAS bf16x4*)p1);
    return __builtin_shufflevector(lo, hi, 0, 1, 2, 3, 4, 5, 6, 7);
}
DI f32x4 mfma16(bf16x8 a, bf16x8 b, f32x4 c) { return __builtin_amdgcn_mfma_f32_16x16x32_bf16(a, b, c, 0, 0, 0); }
DI void mfma_acc(f32x4& acc, bf16x8 a, bf16x8 b) { asm volatile("v_mfma_f32_16x16x32_bf16 %0, %1, %2, %0" : "+v"(acc) : "v"(a), "v"(b)); }
DI float shfl(float v, int src) { return __int_as_float(__builtin_amdgcn_ds_bpermute(src << 2, __float_as_int(v))); }
DI bf16x8 as_frag(u32x4 v) { return __builtin_bit_cast(bf16x8, v); }
DI unsigned pack2(float a, float b) {
    f32x2 f = {a, b};
    bf16x2 h = __builtin_convertvector(f, bf16x2);
    return __builtin_bit_cast(unsigned, h);
}
DI float bf_lo(unsigned u) { return __uint_as_float(u << 16); }
DI float bf_hi(unsigned u) { return __uint_as_float(u & 0xffff0000u); }
DI float dot2(unsigned a, unsigned b, float c) {
    return __builtin_amdgcn_fdot2_f32_bf16(__builtin_bit_cast(bf16x2, a), __builtin_bit_cast(bf16x2, b), c, false);
}
DI float fmax_raw(float a, float b) { float r; asm("v_max_f32 %0, %1, %2" : "=v"(r) : "v"(a), "v"(b)); return r; }
DI float fmax3_raw(float a, float b, float c) { float r; asm("v_max3_f32 %0, %1, %2, %3" : "=v"(r) : "v"(a), "v"(b), "v"(c)); return r; }
DI float relu(float x) { const int i = __float_as_int(x); return __int_as_float(i > 0 ? i : 0); }
DI float reglu(float g, float u) { float p = relu(g) * u; asm("" : "+v"(p)); return p; }
DI float red_sum_g(float v) {
    auto a = __builtin_amdgcn_permlane16_swap(__float_as_uint(v), __float_as_uint(v), false, false);
    v = __uint_as_float(a[0]) + __uint_as_float(a[1]);
    auto b = __builtin_amdgcn_permlane32_swap(__float_as_uint(v), __float_as_uint(v), false, false);
    return __uint_as_float(b[0]) + __uint_as_float(b[1]);
}
DI float red_max_g(float v) {
    auto a = __builtin_amdgcn_permlane16_swap(__float_as_uint(v), __float_as_uint(v), false, false);
    v = fmax_raw(__uint_as_float(a[0]), __uint_as_float(a[1]));
    auto b = __builtin_amdgcn_permlane32_swap(__float_as_uint(v), __float_as_uint(v), false, false);
    return fmax_raw(__uint_as_float(b[0]), __uint_as_float(b[1]));
}
DI int opaque_lane() { int l; asm volatile("v_mbcnt_lo_u32_b32 %0, -1, 0\n\tv_mbcnt_hi_u32_b32 %0, -1, %0" : "=v"(l)); return l; }
DI int opaque_s(int v) { asm volatile("" : "+s"(v)); return v; }

DI float bcast_row0(float v) { return __int_as_float(__builtin_amdgcn_update_dpp(0, __float_as_int(v), 0x150, 0xf, 0xf, false)); }

DI void set_flag(rsrc_t rws, unsigned off, u64 val, int lane) {
    if (lane == 0) st8(rws, 0, WS_FLAGS + off, __builtin_bit_cast(u32x2, val));
    CFENCE();
}
DI void publish(rsrc_t rws, unsigned off, u64 val, int w, int lane) {
    WAIT_V(0);
    BAR();
    if (w == 0) set_flag(rws, off, val, lane);
}
DI u64 flag_ld(rsrc_t rws, unsigned off) { CFENCE(); const u64 v = __builtin_bit_cast(u64, ld8<SC1>(rws, off, WS_FLAGS)); CFENCE(); return v; }
template <bool WITH_BAR = false> DI void poll(rsrc_t rws, unsigned off, u64 lo, unsigned span = 0) {
    u64 v0, v1, v2 = 0;
    v0 = flag_ld(rws, off); __builtin_amdgcn_s_sleep(4);
    v1 = flag_ld(rws, off); __builtin_amdgcn_s_sleep(4);
#pragma unroll 1
    for (unsigned it = 0; it < (1u << 19); ++it) {
        v2 = flag_ld(rws, off); if (__all(v0 - lo <= span)) break;
        v0 = flag_ld(rws, off); if (__all(v1 - lo <= span)) break;
        v1 = flag_ld(rws, off); if (__all(v2 - lo <= span)) break;
    }
    if (WITH_BAR) BAR();
    asm volatile("" :: "v"(v0), "v"(v1), "v"(v2) : "memory");
}

constexpr int A_RING = 92160;
constexpr int A_SCR = 149504, A_SCR_RS = 1168;
constexpr int A_RS = 464;
constexpr int A_ST = 162816;
constexpr int A_MEAN = 163456;
constexpr int B_W1 = 92160, B_W2 = 40960, B_N1 = 65;
constexpr int B_RED = 0;
constexpr unsigned ONES2 = 0x3f803f80u;

struct ACtx { int lane, w, tt, hg; lptr ta0, ta1; unsigned xo, xs[4], xl[4]; u64 xm; };
DI ACtx a_ctx(lptr sm, const int w_) {
    ACtx c;
    c.lane = opaque_lane();
    const int bid = opaque_s(blockIdx.x);
    c.w = opaque_s(w_);
    c.tt = (bid & 7) * 4 + ((bid >> 3) & 3); c.hg = bid >> 5;
    const int t = c.lane & 15, g = c.lane >> 4;
    c.ta0 = sm + t * 128 + ((g ^ (t & 7)) * 16); c.ta1 = sm + t * 128 + (((4 + g) ^ (t & 7)) * 16);
    c.xo = (unsigned)((c.lane >> 3) * 1152 + ((c.lane & 7) ^ (c.lane >> 3)) * 16);
#pragma unroll
    for (int i = 0; i < 4; ++i) {
        int id = i * 8 + c.w - 3; id = id > 26 ? 26 : id;
        int kq = (id >= 18) ? 2 : (id >= 9 ? 1 : 0), rb = 1 + id - kq * 9;
        if (id < 0) { kq = c.w; rb = 0; }
        int r0 = c.tt * 64 - 16 + rb * 8; r0 = r0 < 0 ? r0 + 16 : r0;
        c.xs[i] = (unsigned)(r0 * 1152 + kq * 128);
        c.xl[i] = (unsigned)(size_t)sm + (kq * 10 + rb) * 1024;
    }
    c.xm = c.w < 3 ? ~0xffull : ~0ull;
    return c;
}
DI void a_xdma(const ACtx& c, const srd_t rx, const int third) {
    dma_masked(rx, c.xo, c.xs[0], third * 384, c.xl[0], third * 30720, c.xm);
#pragma unroll
    for (int i = 1; i < 4; ++i) dma<SC1>(rx, c.xo, c.xs[i], third * 384, c.xl[i], third * 30720);
}
DI bf16x8 a_ones_frag() {
    u32x4 o;
#pragma unroll
    for (int e = 0; e < 4; ++e) asm volatile("v_mov_b32 %0, 0x3f803f80" : "=v"(o[e]));
    return as_frag(o);
}
DI void a_ones(lptr sm, const int lane) {
    const bf16x8 o = a_ones_frag();
    if (lane < 8) {
#pragma unroll
        for (int kb = 0; kb < 9; ++kb) *(LAS bf16x8*)(sm + kb * 10240 + lane * 16) = o;
    }
}
DI void a_tok(bf16x8 (&tk)[5], const ACtx& c, const int s) {
    lptr ta = ((s & 1) ? c.ta1 : c.ta0) + (s >> 1) * 10240;
#pragma unroll
    for (int i = 0; i < 5; ++i) tk[i] = lds_frag(ta + i * 2048);
}
DI void a_mm(f32x4 (&acc)[2][5], const bf16x8 (&tk)[5], const bf16x8 f0, const bf16x8 f1, const int s) {
    const f32x4 z = {0.f, 0.f, 0.f, 0.f};
#pragma unroll
    for (int i = 0; i < 5; ++i) {
        if (s == 0) { acc[0][i] = mfma16(f0, tk[i], z); acc[1][i] = mfma16(f1, tk[i], z); }
        else if (s == 17) { acc[0][i] = mfma16(f0, tk[i], acc[0][i]); acc[1][i] = mfma16(f1, tk[i], acc[1][i]); }
        else { mfma_acc(acc[0][i], f0, tk[i]); mfma_acc(acc[1][i], f1, tk[i]); }
    }
}
DI void a_mm_stats(f32x4 (&acc)[2][5], const bf16x8 (&tk)[5], const bf16x8 ones, const int s) {
    const f32x4 z = {0.f, 0.f, 0.f, 0.f};
#pragma unroll
    for (int i = 0; i < 5; ++i) {
        if (s == 0) { acc[0][i] = mfma16(ones, tk[i], z); acc[1][i] = mfma16(tk[i], tk[i], z); }
        else if (s == 17) { acc[0][i] = mfma16(ones, tk[i], acc[0][i]); acc[1][i] = mfma16(tk[i], tk[i], acc[1][i]); }
        else { mfma_acc(acc[0][i], ones, tk[i]); mfma_acc(acc[1][i], tk[i], tk[i]); }
    }
}
DI void a_stats_out(const f32x4 (&acc)[2][5], lptr sm, const int lane) {
    const int t = lane & 15, g = lane >> 4;
    if (g == (t >> 2)) {
#pragma unroll
        for (int i = 0; i < 5; ++i) {
            const float sq = (t & 2) ? ((t & 1) ? acc[1][i][3] : acc[1][i][2]) : ((t & 1) ? acc[1][i][1] : acc[1][i][0]);
            const float mean = acc[0][i][0] * (1.0f / D_);
            const float rstd = __builtin_amdgcn_rsqf(fmaxf(sq * (1.0f / D_) - mean * mean, 0.0f) + LN_EPS);
            *(LAS f32x2*)(sm + A_ST + (16 * i + t) * 8) = (f32x2){mean * rstd, rstd};
            *(LAS float*)(sm + A_MEAN + (16 * i + t) * 4) = mean;
        }
    }
}
struct APre { f32x4 bias[2]; unsigned ow, ost, rb, vb, oo[3]; };
DI void a_pre(APre& p, const ACtx& c) {
    const int lane = c.lane, t = lane & 15, g = lane >> 4, i = c.w >> 1, hp = c.w & 1;
    const int pos = c.tt * 64 + 16 * i + t;
    const unsigned V = 0x5555u & ((2u << (pos < 14 ? pos : 14)) - 1u);
    const int sh = 13 + t - 4 * g;
    const unsigned U[2] = {~(V >> sh), ~((V << 16) >> sh)};
#pragma unroll
    for (int a = 0; a < 2; ++a)
#pragma unroll
        for (int r = 0; r < 4; ++r) p.bias[a][r] = __uint_as_float((unsigned)__builtin_amdgcn_sbfe((int)U[a], 3 - r, 1) & 0xf149f2cau);
    p.ow = (unsigned)(t * A_RS + c.w * 64 + g * 8);
    p.ost = (unsigned)(A_ST + t * 8);
    p.rb = (unsigned)((16 * i + t) * A_RS + hp * 72 + g * 16);
    p.vb = (unsigned)((16 * i + (lane >> 2)) * A_RS + 288 + hp * 72 + (lane & 3) * 8);
#pragma unroll
    for (int k = 0; k < 3; ++k) {
        const int d = c.hg * 72 + hp * 36 + 16 * k + 4 * g;
        p.oo[k] = (unsigned)((d >> 5) * 131072 + ((d >> 3) & 3) * 256 + t * 16 + (d & 7) * 2);
    }
    asm volatile("" : "+v"(p.bias[0]), "+v"(p.bias[1]), "+v"(p.ow), "+v"(p.ost), "+v"(p.rb), "+v"(p.vb), "+v"(p.oo[0]), "+v"(p.oo[1]), "+v"(p.oo[2]));
}

DI void a_gemm1(f32x4 (&acc)[2][5], APre& pre, lptr sm, char* ws, const char* xin, const char* wqkv_, const char* wo_, const int w_, u64& tag) {
    const ACtx c = a_ctx(sm, w_);
    const int lane = c.lane, w = c.w, g = lane >> 4, q = (lane >> 2) & 3, pp = lane & 3;
    const srd_t rx = mk_srd(xin, ACT_BYTES), rw = mk_srd(wqkv_, WQKV_BYTES);
    const rsrc_t rws = mk_rsrc(ws, WS_END);
    const int bid = blockIdx.x;
    { const unsigned o = (unsigned)(bid >> 3) * 62208 + (w * 8192 > 54016 ? 54016 : w * 8192); warm(rw, lane * 128, o, (unsigned)(size_t)sm + 160768 + w * 256); }
    a_xdma(c, rx, 0); a_xdma(c, rx, 1); a_xdma(c, rx, 2);
    asm volatile("" : "+s"(tag));
    bf16x8 tk[2][5];
    if (w == 7) {
        a_ones(sm, lane);
        u32x4 v[9];
        if (bid < 72) {
            const rsrc_t ro = mk_rsrc(wo_, WO_BYTES);
#pragma unroll
            for (int j = 0; j < 9; ++j) v[j] = ld16<0>(ro, (unsigned)((lane >> 3) * 1152 + (lane & 7) * 16), bid * 9216 + j * 128);
            WAIT_V(9);
        } else {
            WAIT_V(0);
        }
        WAIT_L0(); BAR();
        if (bid < 72) {
            float s0 = 0.f, s1 = 0.f;
#pragma unroll
            for (int j = 0; j < 9; ++j) {
                s0 = dot2(v[j][0], ONES2, s0); s1 = dot2(v[j][1], ONES2, s1); s0 = dot2(v[j][2], ONES2, s0); s1 = dot2(v[j][3], ONES2, s1);
                *(LAS u32x4*)(sm + A_SCR + (lane >> 3) * A_SCR_RS + (lane & 7) * 16 + j * 128) = v[j];
            }
            float s = s0 + s1;
            s += shfl(s, lane ^ 1); s += shfl(s, lane ^ 2); s += shfl(s, lane ^ 4);
            s *= (1.0f / D_);
            const float hi = bf_lo(pack2(s, 0.f));
            if ((lane & 7) == 0) *(LAS u32x2*)(sm + A_SCR + (lane >> 3) * A_SCR_RS + 1152) = (u32x2){pack2(hi, s - hi), 0u};
            WAIT_L0();
#pragma unroll
            for (int k = 0; k < 10; ++k) {
                const int f = 4 * k + g, cb = f / 5, j = f - cb * 5;
                lptr p = sm + A_SCR + q * A_SCR_RS + ((j == 4 && pp >= 2) ? 1152 : cb * 144 + j * 32 + pp * 8);
                const bf16x8 fr = lds_tr2(p, p + 4 * A_SCR_RS);
                st16(rws, (unsigned)(cb * 92160 + j * 1024 + (lane & 15) * 16), WS_WOF + (bid >> 2) * 5120 + (bid & 3) * 256, __builtin_bit_cast(u32x4, fr));
            }
            WAIT_V(0);
            set_flag(rws, FL_S * 2048 + bid * 8, tag, lane);
        } else {
            __builtin_amdgcn_s_waitcnt(0x0F70);
        }
        const bf16x8 ones = a_ones_frag();
        a_tok(tk[0], c, 0);
#pragma unroll
        for (int s = 0; s < 18; ++s) {
            if (s + 1 < 18) a_tok(tk[(s + 1) & 1], c, s + 1);
            if (s == 15) a_pre(pre, c);
            SCHED();
            a_mm_stats(acc, tk[s & 1], ones, s);
            SCHED();
        }
        a_stats_out(acc, sm, lane);
    } else {
        unsigned woff[2];
#pragma unroll
        for (int i = 0; i < 2; ++i) {
            const int p = i * 16 + (lane >> 2);
            int cc = 4 * w + ((lane & 3) ^ (((p >> 3) & 1) << 1));
            cc = cc > 26 ? 26 : cc;
            const int mat = (cc >= 18) ? 2 : (cc >= 9 ? 1 : 0);
            woff[i] = (unsigned)(p * 3456 + mat * (1152 - 144) + cc * 16);
        }
        lptr ring = sm + A_RING + w * 8192;
        const unsigned rl = (unsigned)(size_t)ring, hs = c.hg * 144;
        const int wo0 = (8 * g + q) * 64 + ((g & 1) * 32) + pp * 8, wo1 = wo0 ^ 32;
#define A_WDMA(s) do { dma(rw, woff[0], hs, (s) * 32 * 3456, rl, ((s) & 3) * 2048); dma(rw, woff[1], hs, (s) * 32 * 3456, rl, ((s) & 3) * 2048 + 1024); } while (0)
#define A_WRD(d, s) do { lptr sl = ring + ((s) & 3) * 2048; d[0] = lds_tr2(sl + wo0, sl + wo0 + 256); d[1] = lds_tr2(sl + wo1, sl + wo1 + 256); } while (0)
        A_WDMA(0); A_WDMA(1); A_WDMA(2); A_WDMA(3);
        WAIT_V(6); BAR();
        bf16x8 wf[2][2];
        A_WRD(wf[0], 0);
        a_tok(tk[0], c, 0);
#pragma unroll
        for (int s = 0; s < 18; ++s) {
            WAIT_L0();
            if (s + 4 < 18) A_WDMA(s + 4);
            if (s + 1 < 18) {
                if (s + 4 < 18) { WAIT_V(6); } else if (s == 14) { WAIT_V(4); } else if (s == 15) { WAIT_V(2); } else { WAIT_V(0); }
                A_WRD(wf[(s + 1) & 1], s + 1);
                a_tok(tk[(s + 1) & 1], c, s + 1);
            }
            if (s == c.tt) {
                const unsigned o = WS_WQF + ((c.hg * 18 + s) * 14 + 2 * w) * 1024;
                st16(rws, lane * 16, o, __builtin_bit_cast(u32x4, wf[s & 1][0]));
                st16(rws, lane * 16, o + 1024, __builtin_bit_cast(u32x4, wf[s & 1][1]));
            }
            if (s == 15) a_pre(pre, c);
            SCHED();
            a_mm(acc, tk[s & 1], wf[s & 1][0], wf[s & 1][1], s);
            SCHED();
        }
#undef A_WDMA
#undef A_WRD
    }
}

DI void a_gemm2(f32x4 (&acc)[2][5], APre& pre, const bf16x8 (&we)[18][2], lptr sm, char* ws, const int w_, u64 tag) {
    asm volatile("" : "+s"(tag));
#pragma unroll
    for (int s = 0; s < 18; ++s) asm volatile("" :: "v"(we[s][0]), "v"(we[s][1]));
    const ACtx c = a_ctx(sm, w_);
    const int lane = c.lane, w = c.w;
    const srd_t rx = mk_srd(ws + WS_X2, ACT_BYTES), rs = mk_srd(ws, WS_END);
    const unsigned l16 = lane * 16, wsb = WS_WOF + c.hg * 92160 + w * 1024, wlb = (unsigned)(size_t)sm + B_W1 + w * 1024;
    if (w == 7) {
        a_ones(sm, lane);
        int i = 8 * c.tt - 4 + (lane < 12 ? lane : 11); i = i < 0 ? 0 : i;
        WAIT_L0();
        poll<true>(mk_rsrc(ws, WS_END), FL_E * 2048 + i * 8, tag);
    } else {
        SCHED();
        BAR();
    }
    a_xdma(c, rx, 0); a_xdma(c, rx, 1);
    bf16x8 tk[2][5];
    WAIT_V(4); BAR();
#define A_LOOP(MM) \
    a_tok(tk[0], c, 0); \
    _Pragma("unroll") for (int s = 0; s < 18; ++s) { \
        if (s == 5) { WAIT_V(4); BAR(); } \
        if (s == 11) { WAIT_V(5); BAR(); } \
        if (s + 1 < 18) a_tok(tk[(s + 1) & 1], c, s + 1); \
        if (s == 1) a_xdma(c, rx, 2); \
        if (s >= 6 && s < 14) dma<SC1>(rs, l16, wsb, (s - 6) * 8192, wlb, (s - 6) * 8192); \
        if (s == 14) { if (w == 0) dma<SC1>(rs, l16, wsb, 65536, wlb, 65536); } \
        if (s == 15) a_pre(pre, c); \
        SCHED(); \
        MM; \
        SCHED(); \
    }
    if (w == 7) { const bf16x8 ones = a_ones_frag(); A_LOOP(a_mm_stats(acc, tk[s & 1], ones, s)) a_stats_out(acc, sm, lane); }
    else { A_LOOP(a_mm(acc, tk[s & 1], we[s][0], we[s][1], s)) }
#undef A_LOOP
}

DI void a_post(f32x4 (&acc)[2][5], const APre& pre, lptr sm, char* ws, const char* wd_, const int l_, const int w_, u64 tag) {
    asm volatile("" : "+s"(tag));
    const int lane = opaque_lane();
    const int bid = opaque_s(blockIdx.x), w = opaque_s(w_), l = opaque_s(l_);
    const int tt = (bid & 7) * 4 + ((bid >> 3) & 3), hg = bid >> 5;
    const int g = lane >> 4;
    const rsrc_t rws = mk_rsrc(ws, WS_END);
    f32x2 csb[2][2];
#pragma unroll
    for (int j = 0; j < 2; ++j)
#pragma unroll
        for (int r = 0; r < 4; ++r) csb[j][r >> 1][r & 1] = bcast_row0(acc[j][0][r]);
    WAIT_L0();
    SCHED();
    BAR();
    if (w < 7) {
        f32x2 st[5];
#pragma unroll
        for (int i = 0; i < 5; ++i) st[i] = *(const LAS f32x2*)(sm + pre.ost + 128 * i);
        SCHED();
#pragma unroll
        for (int i = 0; i < 5; ++i) {
#pragma unroll
            for (int j = 0; j < 2; ++j) {
                const f32x2 a = (f32x2){acc[j][i][0], acc[j][i][1]} * st[i][1] - csb[j][0] * st[i][0];
                const f32x2 b = (f32x2){acc[j][i][2], acc[j][i][3]} * st[i][1] - csb[j][1] * st[i][0];
                *(LAS u32x2*)(sm + pre.ow + 16 * i * A_RS + j * 32) = (u32x2){pack2(a[0], a[1]), pack2(b[0], b[1])};
            }
        }
    } else if (l == 0) {
        const u32x4 fv = ld16<SC1>(rws, (lane < 36 ? lane : 0) * 16, WS_FLAGS + FL_S * 2048);
        const u32x2 tg = __builtin_bit_cast(u32x2, tag);
        if (!__all(fv[0] == tg[0] && fv[1] == tg[1] && fv[2] == tg[0] && fv[3] == tg[1])) {
            poll(rws, FL_S * 2048 + lane * 8, tag);
            poll(rws, FL_S * 2048 + (64 + (lane & 7)) * 8, tag);
        }
        CFENCE();
    } else {
        const srd_t rs = mk_srd(ws, WS_END);
        const unsigned sb = WS_WOF + hg * 92160 + B_N1 * 1024, lb = (unsigned)(size_t)sm + B_W2;
#pragma unroll
        for (int k = 0; k < 90 - B_N1; ++k) dma<SC1>(rs, lane * 16, sb, k * 1024, lb, k * 1024);
    }
    WAIT_L0();
    BAR();
    {
        lptr rb = sm + pre.rb;
        u32x4 qf[2]; bf16x8 kf[2][2], vf[3];
#pragma unroll
        for (int hh = 0; hh < 2; ++hh) {
            const u32x2 a = *(const LAS u32x2*)(rb + 16 * A_RS + hh * 8), b = *(const LAS u32x2*)(rb + 16 * A_RS + hh * 8 + 8);
            qf[hh] = (u32x4){a[0], a[1], b[0], b[1]};
#pragma unroll
            for (int k = 0; k < 2; ++k) {
                const u32x2 c = *(const LAS u32x2*)(rb + 144 + 16 * k * A_RS + hh * 8), d = *(const LAS u32x2*)(rb + 144 + 16 * k * A_RS + hh * 8 + 8);
                kf[hh][k] = as_frag((u32x4){c[0], c[1], d[0], d[1]});
            }
        }
        lptr vb = sm + pre.vb;
#pragma unroll
        for (int c = 0; c < 3; ++c) vf[c] = lds_tr2(vb + 32 * c, vb + 32 * c + 16 * A_RS);
        SCHED();
        qf[0][0] = g < 3 ? qf[0][0] : 0u; qf[0][1] = g < 2 ? qf[0][1] : 0u; qf[0][2] = g < 2 ? qf[0][2] : 0u; qf[0][3] = g < 2 ? qf[0][3] : 0u;
        qf[1][0] = g > 1 ? qf[1][0] : 0u; qf[1][1] = g > 1 ? qf[1][1] : 0u; qf[1][2] = g > 1 ? qf[1][2] : 0u; qf[1][3] = g > 0 ? qf[1][3] : 0u;
        f32x4 sc[2][2];
#pragma unroll
        for (int hh = 0; hh < 2; ++hh)
#pragma unroll
            for (int k = 0; k < 2; ++k) sc[hh][k] = mfma16(kf[hh][k], as_frag(qf[hh]), pre.bias[k]);
        constexpr float kScale = 0.23570226039551584f * 1.4426950408889634f;
        float mx[2], sum[2];
#pragma unroll
        for (int hh = 0; hh < 2; ++hh) {
            const float m0 = fmaxf(fmaxf(sc[hh][0][0], sc[hh][0][1]), fmaxf(sc[hh][0][2], sc[hh][0][3]));
            const float m1 = fmaxf(fmaxf(sc[hh][1][0], sc[hh][1][1]), fmaxf(sc[hh][1][2], sc[hh][1][3]));
            mx[hh] = fmaxf(m0, m1);
        }
        mx[0] = red_max_g(mx[0]) * -kScale; mx[1] = red_max_g(mx[1]) * -kScale;
#pragma unroll
        for (int hh = 0; hh < 2; ++hh) {
#pragma unroll
            for (int k = 0; k < 2; ++k)
#pragma unroll
                for (int r = 0; r < 4; ++r) sc[hh][k][r] = __builtin_amdgcn_exp2f(__builtin_fmaf(sc[hh][k][r], kScale, mx[hh]));
            const f32x4 s4 = sc[hh][0] + sc[hh][1];
            sum[hh] = (s4[0] + s4[1]) + (s4[2] + s4[3]);
        }
        sum[0] = __builtin_amdgcn_rcpf(red_sum_g(sum[0])); sum[1] = __builtin_amdgcn_rcpf(red_sum_g(sum[1]));
        f32x4 o[2][2];
#pragma unroll
        for (int hh = 0; hh < 2; ++hh) {
            const f32x4 p0 = sc[hh][0] * sum[hh], p1 = sc[hh][1] * sum[hh];
            const u32x4 pf = {pack2(p0[0], p0[1]), pack2(p0[2], p0[3]), pack2(p1[0], p1[1]), pack2(p1[2], p1[3])};
            o[hh][0] = mfma16(vf[hh], as_frag(pf), (f32x4){0.f, 0.f, 0.f, 0.f});
            o[hh][1] = mfma16(vf[hh + 1], as_frag(pf), (f32x4){0.f, 0.f, 0.f, 0.f});
        }
        const unsigned ob = WS_O + l * ACT_BYTES + (tt * 4 + (w >> 1)) * 1024;
        st8(rws, pre.oo[0], ob, (u32x2){pack2(o[0][0][0], o[0][0][1]), pack2(o[0][0][2], o[0][0][3])});
        const unsigned e = pack2(o[0][1][0], o[0][1][1]), f = pack2(o[1][0][0], o[1][0][1]);
        st8(rws, pre.oo[1], ob, (u32x2){g == 0 ? e : f, pack2(o[1][0][2], o[1][0][3])});
        if (g == 0) st8(rws, pre.oo[2], ob, (u32x2){pack2(o[1][1][0], o[1][1][1]), pack2(o[1][1][2], o[1][1][3])});
    }
    publish(rws, (FL_A + l) * 2048 + (tt * 8 + hg) * 8, tag, w, lane);
    (void)wd_;
}

DI void stage_b(lptr sm, char* ws, const char* xin, const int l_, const int w_, u64 tag) {
    asm volatile("" : "+s"(tag));
    const int lane = opaque_lane();
    const int bid = opaque_s(blockIdx.x), w = opaque_s(w_), l = opaque_s(l_);
    const int tt = (bid & 7) * 4 + ((bid >> 3) & 3), cb = bid >> 5;
    const int t = lane & 15, g = lane >> 4;
    const int ti = w & 3, kh = w >> 2;
    const int token = tt * 64 + ti * 16 + t;
    const rsrc_t rws = mk_rsrc(ws, WS_END), rx = mk_rsrc(xin, ACT_BYTES);
    const unsigned l16 = lane * 16;

    u32x2 res[3];
    {
        const unsigned o = (unsigned)(token * 1152 + g * 8);
        res[0] = ld8<SC1>(rx, o, cb * 144 + kh * 64);
        res[1] = ld8<SC1>(rx, o + 32, cb * 144 + kh * 64);
        res[2] = ld8<SC1>(rx, o + ((g < 2) ? 128 : 0), cb * 144);
    }
    if (l == 0) {
        const srd_t rs = mk_srd(ws, WS_END);
        const unsigned sb = WS_WOF + cb * 92160 + w * 1024, lb = (unsigned)(size_t)sm + w * 1024;
#pragma unroll
        for (int k = 0; k < 8; ++k) dma<SC1>(rs, l16, sb, k * 8192, lb, B_W1 + k * 8192);
#pragma unroll
        for (int k = 0; k < 3; ++k) dma<SC1>(rs, l16, sb, (B_N1 + k * 8) * 1024, lb, B_W2 + k * 8192);
        if (w == 0) { dma<SC1>(rs, l16, sb, 65536, lb, B_W1 + 65536); dma<SC1>(rs, l16, sb, 89 * 1024, lb, B_W2 + 24576); }
        WAIT_V(0); BAR();
    }
    bf16x8 wf[9][5];
    {
        lptr ba = sm + B_W1 + kh * 46080 + l16, bb = sm + (kh ? B_W2 - 20480 : B_W1) + l16;
#pragma unroll
        for (int s = 0; s < 9; ++s)
#pragma unroll
            for (int j = 0; j < 5; ++j) wf[s][j] = lds_frag((s < 4 ? ba : bb) + (s * 5 + j) * 1024);
    }
    f32x4 r0[3]; unsigned xo[3];
    float xmean = *(const LAS float*)(sm + A_MEAN + (16 + ti * 16 + t) * 4);
#pragma unroll
    for (int j = 0; j < 3; ++j) {
        r0[j] = (f32x4){bf_lo(res[j][0]), bf_hi(res[j][0]), bf_lo(res[j][1]), bf_hi(res[j][1])} - xmean;
        const int d = cb * 72 + (j < 2 ? 32 * kh + 16 * j : 64) + 4 * g;
        xo[j] = (unsigned)((d >> 5) * 131072 + ((d >> 3) & 3) * 256 + t * 16 + (d & 7) * 2);
        asm volatile("" : "+v"(r0[j]), "+v"(xo[j]));
    }
    unsigned so = (unsigned)(token * 64), bsrc = (unsigned)((32 + t) * 4);
    asm volatile("" : "+v"(so), "+v"(bsrc), "+v"(xmean));
    SCHED();
    if (w == 7) poll<true>(rws, (FL_A + l) * 2048 + (tt * 8 + (lane & 7)) * 8, tag);
    else BAR();
    bf16x8 of[9];
    const unsigned ob = WS_O + l * ACT_BYTES + (kh * 9 * 128 + tt * 4 + ti) * 1024;
#pragma unroll
    for (int s = 0; s < 9; ++s) of[s] = as_frag(ld16<SC1>(rws, l16, ob + s * 131072));
    CFENCE(); SCHED();
    f32x4 acc[5];
#pragma unroll
    for (int s = 0; s < 9; ++s) {
#pragma unroll
        for (int j = 0; j < 5; ++j) acc[j] = mfma16(wf[s][j], of[s], s ? acc[j] : (f32x4){0.f, 0.f, 0.f, 0.f});
        SCHED();
    }
    f32x4 own[3];
    {
        lptr rd = sm + B_RED + w * 3072 + l16;
        if (kh) { *(LAS f32x4*)(rd) = acc[0]; *(LAS f32x4*)(rd + 1024) = acc[1]; own[0] = acc[2] + r0[0]; own[1] = acc[3] + r0[1]; }
        else    { *(LAS f32x4*)(rd) = acc[2]; *(LAS f32x4*)(rd + 1024) = acc[3]; own[0] = acc[0] + r0[0]; own[1] = acc[1] + r0[1]; }
        *(LAS f32x4*)(rd + 2048) = acc[4];
        own[2] = acc[4];
    }
    WAIT_L0();
    BAR();
    {
        lptr rd = sm + B_RED + (w ^ 4) * 3072 + l16;
        const f32x4 p2 = *(const LAS f32x4*)(rd + 2048), p0 = *(const LAS f32x4*)(rd), p1 = *(const LAS f32x4*)(rd + 1024);
        own[2] += p2;
        const float dm = __int_as_float(__builtin_amdgcn_ds_bpermute(bsrc, __float_as_int(own[2][0] + own[2][1])));
        own[0] += p0; own[1] += p1; own[2] += r0[2];
        float sq = 0.f;
        const unsigned xb = WS_X1 + l * ACT_BYTES + (tt * 4 + ti) * 1024;
#pragma unroll
        for (int j = 0; j < 3; ++j) {
            const f32x4 v = own[j] - dm;
            const u32x2 pk = {pack2(v[0], v[1]), pack2(v[2], v[3])};
            if (j < 2 || (g < 2 && kh == 0)) {
                st8(rws, xo[j], xb, pk);
                sq = dot2(pk[0], pk[0], sq); sq = dot2(pk[1], pk[1], sq);
            }
        }
        sq = red_sum_g(sq);
        if (g == 0) st4(rws, so, WS_STATS + l * 131072 + cb * 8 + kh * 4, __float_as_uint(sq));
        if (cb == 0 && kh == 0) { if (g == 1) st4(rws, so >> 4, WS_MEAN + l * 8192, __float_as_uint(xmean + dm)); }
    }
    WAIT_L0();
    publish(rws, (FL_B + l) * 2048 + (tt * 8 + cb) * 8, tag, w, lane);
}

#define EARLY 1
#define WARM1 1
#define WARM2 1
constexpr int C_BUF = 45056;
constexpr int C_S2 = 90112, C_S3 = 126976;
#define C_SLOT(b) (((b) & 1) ? (((b) & 2) ? C_S3 : C_S2) : (((b) & 2) ? C_BUF : 0))
#define E_FETCH(k, slot) do { _Pragma("unroll") for (int f_ = 0; f_ < 4; ++f_) \
        dma<SC1>(rx, evo, esb, f_ * 4 * (int)ACT_BYTES + (k) * 2048, elb, (slot) + f_ * 1024); } while (0)

struct EPre { float rsq; f32x4 r0[3]; unsigned xo[3]; };

DI void stage_c(EPre& ep, lptr sm, char* ws, const char* wg_, const char* wu_, const char* wd_, const int l_, const int w_, u64 tag) {
    asm volatile("" : "+s"(tag));
    const int lane = opaque_lane();
    const int bid = opaque_s(blockIdx.x), w = opaque_s(w_), l = opaque_s(l_);
    const int fs = 2 * (bid & 7) + ((bid >> 3) & 1), T = bid >> 4;
    const int m = w >> 2, n = w & 3;
    const int g = lane >> 4;
    const int q = (lane >> 2) & 3, pp = lane & 3;
    const rsrc_t rws = mk_rsrc(ws, WS_END);
    const srd_t rx = mk_srd(ws, WS_END), rg = mk_srd(wg_, WFF_BYTES), ru = mk_srd(wu_, WFF_BYTES), rd = mk_srd(wd_, WFF_BYTES);
    const srd_t rs = mk_srd((n & 1) ? wu_ : wg_, WFF_BYTES);
    const unsigned smb = (unsigned)(size_t)sm;

    unsigned so[3];
    const int i0 = (1 - m) * 8 + 2 * n;
#pragma unroll
    for (int k = 0; k < 3; ++k) {
        const int ins = (k < 2) ? i0 + k : 16 + (n >> 1);
        const int qq = ins * 64 + lane;
        const int p = qq / 36, c = qq - p * 36;
        so[k] = (unsigned)(p * 9216 + (c ^ (((p >> 3) & 1) << 1)) * 16 + fs * 576);
    }
    const unsigned lw = smb + i0 * 1024, lx = smb + (n & 1) * 18432 + (16 + (n >> 1)) * 1024, lt = smb + 36864 + w * 1024;
    const unsigned l16 = lane * 16 + WS_X1 + l * ACT_BYTES + (T * 8 + w) * 1024;
    const unsigned sd = (unsigned)((lane >> 3) * 1152 + ((lane & 7) ^ (((lane >> 4) & 3) << 1)) * 16);
    const int d0 = m ? 4 * n : 16 + 5 * n;
    const unsigned dsb = (unsigned)((fs * 288 + d0 * 8) * 1152), dlb = smb + d0 * 1024;

#define C_W(k, ks, ts, buf) do { \
        if ((k) == 0) dma(rg, so[0], ks, 0, lw + (buf), 0); \
        if ((k) == 1) dma(ru, so[0], ks, 0, lw + (buf), 18432); \
        if ((k) == 2) dma(rg, so[1], ks, 0, lw + (buf), 1024); \
        if ((k) == 3) dma(ru, so[1], ks, 0, lw + (buf), 18432 + 1024); \
        if ((k) == 4) { if (m == 0) dma(rs, so[2], ks, 0, lx + (buf), 0); } \
        if ((k) == 5) dma<SC1>(rx, l16, ts, 0, lt + (buf), 0); \
        } while (0)
#define C_D(k, b, unused, buf) do { \
        if ((k) < 4) dma(rd, sd, dsb, (k) * 9216 + (b) * 128, dlb, (buf) + (k) * 1024); \
        if ((k) == 4) { if (m == 0) dma(rd, sd, dsb, (k) * 9216 + (b) * 128, dlb, (buf) + (k) * 1024); } \
        } while (0)
#define C_DALL(b, buf) do { C_D(0, b, 0, buf); C_D(1, b, 0, buf); C_D(2, b, 0, buf); C_D(3, b, 0, buf); C_D(4, b, 0, buf); } while (0)
#define C_NONE(k, a, b, buf) do { } while (0)

    if (w != 7) {
#pragma unroll
        for (int k = 0; k < 5; ++k) { C_W(k, 0u, 0u, 0); }
#pragma unroll
        for (int k = 0; k < 5; ++k) { C_W(k, 294912u, 0u, C_BUF); }
    }
    const int rowoff = (8 * g + q) * 576 + pp * 8;
    int be = rowoff + 128 * n + 32 * (g & 1);
    int bo = rowoff + 128 * n - 32 * (g & 1);
    int bm = (8 * g + q) * 576 + (((32 + n) ^ (2 * (g & 1))) * 16) + 8 * (pp & 1) + (pp >> 1) * 18432;
    int bt = 36864 + m * 4096 + lane * 16;
    asm volatile("" : "+v"(be), "+v"(bo), "+v"(bm), "+v"(bt));
    f32x4 acc[9][4];
#pragma unroll
    for (int j = 0; j < 9; ++j)
#pragma unroll
        for (int i = 0; i < 4; ++i) {
            f32x2 z0, z1;
            asm volatile("v_mov_b64 %0, 0\n\tv_mov_b64 %1, 0" : "=v"(z0), "=v"(z1));
            acc[j][i] = __builtin_shufflevector(z0, z1, 0, 1, 2, 3);
        }
    if (w == 7) {
        const unsigned fb = (FL_B + l) * 2048 + (T * 16 + (lane & 15)) * 8, fa = FL_A * 2048 + ((lane - 16) * 8 + (bid >> 5)) * 8;
        poll<true>(rws, (lane >= 16 && lane < 34) ? fa : fb, tag);
    } else {
        BAR();
    }
    C_W(5, 0u, 0u, 0);
    if (w == 7) {
#pragma unroll
        for (int k = 0; k < 4; ++k) { C_W(k, 0u, 0u, 0); }
#pragma unroll
        for (int k = 0; k < 4; ++k) { C_W(k, 294912u, 0u, C_BUF); }
    }
    C_W(5, 0u, 131072u, C_BUF);

#define LORD 0
#define C_RT() do { _Pragma("unroll") for (int i = 0; i < 4; ++i) tk[i] = lds_frag(b + bt + i * 1024); CFENCE(); } while (0)
#define C_RW(j) do { lptr p = b + (((j) & 1) ? bo : be) + 32 * (j); wf[j] = lds_tr2(p, p + 2304); wf[4 + (j)] = lds_tr2(p + 18432, p + 18432 + 2304); CFENCE(); } while (0)
#define C_RM() do { wf[8] = lds_tr2(b + bm, b + bm + 2304); CFENCE(); } while (0)
#define C_LOAD1(cur, ST, a0, a1, a2) do { \
        lptr b = sm + (cur); \
        if (LORD == 0) { ST(0, a0, a1, a2); C_RT(); ST(1, a0, a1, a2); C_RW(0); ST(2, a0, a1, a2); C_RW(1); ST(3, a0, a1, a2); C_RW(2); ST(4, a0, a1, a2); C_RW(3); ST(5, a0, a1, a2); C_RM(); } \
        if (LORD == 1) { ST(0, a0, a1, a2); C_RT(); ST(1, a0, a1, a2); C_RW(0); C_RW(1); ST(2, a0, a1, a2); C_RW(2); C_RW(3); ST(3, a0, a1, a2); C_RM(); ST(5, a0, a1, a2); ST(4, a0, a1, a2); } \
        if (LORD == 2) { ST(0, a0, a1, a2); ST(1, a0, a1, a2); C_RT(); C_RW(0); ST(2, a0, a1, a2); C_RW(1); C_RW(2); ST(3, a0, a1, a2); C_RW(3); C_RM(); ST(5, a0, a1, a2); ST(4, a0, a1, a2); } \
        if (LORD == 3) { ST(0, a0, a1, a2); C_RT(); ST(1, a0, a1, a2); C_RW(0); ST(2, a0, a1, a2); C_RW(1); C_RW(2); ST(3, a0, a1, a2); C_RW(3); ST(5, a0, a1, a2); C_RM(); ST(4, a0, a1, a2); } \
    } while (0)
#define C_STEP1(cur, ST, a0, a1, a2, NX, NY, EXTRA) do { \
        bf16x8 tk[4], wf[9]; \
        C_LOAD1(cur, ST, a0, a1, a2); \
        EXTRA; \
        if (m == 1) WAIT_V(NY); \
        WAIT_L0(); SCHED(); BAR(); \
        __builtin_amdgcn_s_setprio(1); \
        _Pragma("unroll") for (int j = 0; j < 9; ++j) \
            _Pragma("unroll") for (int i = 0; i < 4; ++i) mfma_acc(acc[j][i], wf[j], tk[i]); \
        __builtin_amdgcn_s_setprio(0); \
        SCHED(); \
        if (m == 0) WAIT_V(NX); \
        BAR(); \
    } while (0)

    WAIT_V(1);
    BAR();
    if (m == 1) BAR();
    unsigned l128 = lane * 128;
    const unsigned wdo = (unsigned)(fs * 331776 + T * 20736), wqo = WS_WQF + (bid >> 5) * 258048 + ((bid >> 3) & 3) * 64512;
    int cur = 0;
#pragma unroll 1
    for (unsigned s = 2; s < 18; ++s) {
        const int nxt2 = (cur == 0) ? 2 * C_BUF : cur - C_BUF;
        C_STEP1(cur, C_W, s * 294912u, s * 131072u, nxt2, 6, 5,
                if (w == 0) {
                    if (WARM1 && s < 5) warm(rd, l128, wdo + (s < 4 ? (s - 2) * 8192 : 12544), smb + 135168);
                    if (WARM2 && s >= 10 && l == 0) warm(rx, l128, wqo + (s < 17 ? (s - 10) * 8192 : 56320), smb + 135168);
                });
        cur = (cur == 2 * C_BUF) ? 0 : cur + C_BUF;
    }
    C_STEP1(C_BUF, C_D, 0, 0, 0, 5, 4, );
    lptr hb = sm + (m ? C_S3 : C_BUF);
    const int lane2 = opaque_lane();
    const int t2 = lane2 & 15, g2 = lane2 >> 4;
    {
        bf16x8 tk[4], wf[9];
        C_LOAD1(2 * C_BUF, C_NONE, 0, 0, 0);
        lptr hm = hb + ((g2 >> 1) * 9 + 8) * 1024 + (2 * (n & 1) + (g2 & 1)) * 256 + t2 * 16 + (n >> 1) * 8;
        lptr hw = hb + 2 * n * 1024 + lane2 * 16;
        asm volatile("" : "+v"(hm), "+v"(hw));
        if (m == 1) WAIT_V(0);
        WAIT_L0(); SCHED(); BAR();
        __builtin_amdgcn_s_setprio(1);
#pragma unroll
        for (int i = 0; i < 4; ++i) { acc[8][i] = mfma16(wf[8], tk[i], acc[8][i]); SCHED(); if (m == 1) C_D(i, 1, 0, C_S2); SCHED(); }
        u32x4 o;
        float p0 = 0.f, p1 = 0.f;
#pragma unroll
        for (int z = 0; z < 37; ++z) {
            const int k = z >> 2, e = z & 3;
            if (k < 8) { const int j = 2 * (k >> 2) + (e >> 1) + 4 * (e & 1), i = k & 3; acc[j][i] = mfma16(wf[j], tk[i], acc[j][i]); }
            SCHED();
            if (z > 0) {
                const int kp = (z - 1) >> 2, ep = (z - 1) & 3;
                o[ep] = pack2(p0, p1);
                if (ep == 3) {
                    if (kp == 0) { *(LAS u32x2*)(hm) = (u32x2){o[0], o[1]}; *(LAS u32x2*)(hm + 18432) = (u32x2){o[2], o[3]}; }
                    else *(LAS u32x4*)(hw + (((kp - 1) & 3) * 9 + ((kp - 1) >> 2)) * 1024) = o;
                }
            }
            SCHED();
            if (k == 0) {
                const int i = e & 2, r = 2 * (e & 1);
                auto x = __builtin_amdgcn_permlane32_swap(__float_as_uint(acc[8][i][r]), __float_as_uint(acc[8][i + 1][r]), false, false);
                auto y = __builtin_amdgcn_permlane32_swap(__float_as_uint(acc[8][i][r + 1]), __float_as_uint(acc[8][i + 1][r + 1]), false, false);
                p0 = reglu(__uint_as_float(x[0]), __uint_as_float(x[1])); p1 = reglu(__uint_as_float(y[0]), __uint_as_float(y[1]));
            } else if (k < 9) {
                const int j = 2 * ((k - 1) >> 2) + (e >> 1), i = (k - 1) & 3, r = 2 * (e & 1);
                p0 = reglu(acc[j][i][r], acc[4 + j][i][r]); p1 = reglu(acc[j][i][r + 1], acc[4 + j][i][r + 1]);
            }
            SCHED();
        }
        __builtin_amdgcn_s_setprio(0);
        if (m == 0) WAIT_V(0);
        WAIT_L0(); SCHED(); BAR();
    }
    bf16x8 hf[4][9];
    const int bd = (lane2 >> 2) * 128 + ((n ^ ((lane2 >> 3) & 3)) * 32) + (lane2 & 3) * 8;
    const unsigned po = (unsigned)((t2 >> 2) * 9216 + g2 * 64 + (t2 & 3) * 16);
    const unsigned pb = WS_PART + (l * 16 + fs) * ACT_BYTES + (T * 2 + m) * 73728 + n * 256;
    const unsigned evo = (unsigned)g2 * ACT_BYTES + t2 * 16, esb = WS_PART + l * 16 * ACT_BYTES + (T * 16 + fs) * 9216 + w * 256, elb = smb + w * 4096;
    const unsigned fme = (FL_C + l) * 2048 + (T * 16 + fs) * 8, fpeer = (FL_C + l) * 2048 + (T * 16 + t2) * 8;
    u64 chk = 0;
    u32x2 esv, eres[3]; unsigned emean;
    f32x4 a2[4];
#define C_STORE(b) do { \
        _Pragma("unroll") for (int ip = 0; ip < 2; ++ip) { \
            u32x4 o; \
            o[0] = pack2(a2[2 * ip][0], a2[2 * ip][1]); o[1] = pack2(a2[2 * ip][2], a2[2 * ip][3]); \
            o[2] = pack2(a2[2 * ip + 1][0], a2[2 * ip + 1][1]); o[3] = pack2(a2[2 * ip + 1][2], a2[2 * ip + 1][3]); \
            st16(rws, po, pb + ip * 36864 + (b) * 1024, o); } \
        CFENCE(); \
    } while (0)
#pragma unroll
    for (int b = 0; b < 9; ++b) {
        bf16x8 wf[9];
        lptr pw = sm + C_SLOT(b) + bd;
        if (b == 0) {
            if (m == 0) C_DALL(1, C_S2); else C_DALL(2, C_SLOT(2));
#pragma unroll
            for (int s = 0; s < 9; ++s) wf[s] = lds_tr2(pw + s * 4096, pw + s * 4096 + 2048);
#pragma unroll
            for (int s = 0; s < 9; ++s)
#pragma unroll
                for (int i = 0; i < 4; ++i) hf[i][s] = lds_frag(hb + lane2 * 16 + (i * 9 + s) * 1024);
        } else {
            if (EARLY && b == 7) { if (w == 0) set_flag(rws, fme, tag + 1, lane2); }
            if (b == 1) C_DALL(3, C_SLOT(3));
#pragma unroll
            for (int s = 0; s < 9; ++s) {
                if (b + 3 < 9 && (s & 1) == 0) C_D(s >> 1, b + 3, 0, C_SLOT(b + 3));
                wf[s] = lds_tr2(pw + s * 4096, pw + s * 4096 + 2048);
                CFENCE();
            }
            if (b == 6) {
                const int R = T * 16 + fs, tok = (R >> 2) * 32 + (R & 3) * 4 + (t2 & 3) + (g2 & 1) * 16;
                esv = ld8<SC1>(rws, (unsigned)(tok * 64 + ((t2 >> 2) + 4 * (g2 >> 1)) * 8), WS_STATS + l * 131072);
                emean = ld4<SC1>(rws, (unsigned)(tok * 4), WS_MEAN + l * 8192);
#pragma unroll
                for (int k = 0; k < 3; ++k) {
                    const int bn = k < 2 ? 16 * k + 8 * (g2 >> 1) + w : 32 + n;
                    const int d = 16 * bn + 4 * (t2 >> 2);
                    ep.xo[k] = (unsigned)(tok * 1152 + d * 2);
                    eres[k] = ld8<SC1>(rws, (unsigned)((d >> 5) * 131072 + (tok >> 4) * 1024 + ((d >> 3) & 3) * 256 + (tok & 15) * 16 + (d & 7) * 2), WS_X1 + l * ACT_BYTES);
                    asm volatile("" : "+v"(ep.xo[k]));
                }
                CFENCE();
            }
            if (b == 8) {
                float sq = __uint_as_float(esv[0]) + __uint_as_float(esv[1]);
                sq += shfl(sq, lane2 ^ 4); sq += shfl(sq, lane2 ^ 8); sq += shfl(sq, lane2 ^ 32);
                ep.rsq = __builtin_amdgcn_rcpf(sq * (1.0f / D_) + LN_EPS);
#pragma unroll
                for (int k = 0; k < 3; ++k) {
                    ep.r0[k] = (f32x4){bf_lo(eres[k][0]), bf_hi(eres[k][0]), bf_lo(eres[k][1]), bf_hi(eres[k][1])} + __uint_as_float(emean);
                    asm volatile("" : "+v"(ep.r0[k]));
                }
                asm volatile("" : "+v"(ep.rsq));
            }
            C_STORE(b - 1);
            if (EARLY && b == 8) { if (w == 7) { if (!__all(chk - (tag + 1) <= 1)) poll(rws, fpeer, tag + 1, 1); } }
        }
        if (m == 1) {
            if (b == 0) WAIT_V(4); else if (b == 1) WAIT_V(10); else if (b == 2) WAIT_V(12); else if (b >= 3 && b <= 5) WAIT_V(14); else if (b == 6) WAIT_V(8); else if (b == 7) WAIT_V(6);
        }
        WAIT_L0();
        SCHED();
        BAR();
        if (b == 0) { if (m == 0) C_DALL(2, C_SLOT(2)); }
        if (EARLY && b == 7) { if (w == 7) chk = flag_ld(rws, fpeer); }
        __builtin_amdgcn_s_setprio(1);
#pragma unroll
        for (int i = 0; i < 4; ++i) a2[i] = mfma16(wf[0], hf[i][0], (f32x4){0.f, 0.f, 0.f, 0.f});
#pragma unroll
        for (int s = 1; s < 9; ++s)
#pragma unroll
            for (int i = 0; i < 4; ++i) a2[i] = mfma16(wf[s], hf[i][s], a2[i]);
        __builtin_amdgcn_s_setprio(0);
        SCHED();
        if (m == 0) {
            if (b == 0) WAIT_V(5); else if (b == 1) WAIT_V(12); else if (b == 2) WAIT_V(14); else if (b >= 3 && b <= 5) WAIT_V(16); else if (b == 6) WAIT_V(9); else if (b == 7) WAIT_V(6);
        }
        if (b < 8 || m == 0) BAR();
    }
    C_STORE(8);
    if (EARLY && w != 7) { E_FETCH(0, C_SLOT(5)); E_FETCH(1, C_SLOT(6)); WAIT_V(8); } else { WAIT_V(0); }
    BAR();
    if (w == 7) set_flag(rws, fme, tag + 2, lane2);
#undef C_W
#undef C_D
#undef C_DALL
#undef C_NONE
#undef C_LOAD1
#undef C_STEP1
#undef C_STORE
}

constexpr int E_N1 = 12;

DI void stage_e(bf16x8 (&we)[18][2], const EPre& ep, lptr sm, char* ws, char* xout, const int l_, const int w_, u64 tag) {
    asm volatile("" : "+s"(tag));
    const int lane = opaque_lane();
    const int bid = opaque_s(blockIdx.x), w = opaque_s(w_), l = opaque_s(l_);
    const int fs = 2 * (bid & 7) + ((bid >> 3) & 1), T = bid >> 4;
    const int R = T * 16 + fs;
    const int t = lane & 15, g = lane >> 4;
    const rsrc_t rws = mk_rsrc(ws, WS_END), ro = mk_rsrc(xout, ACT_BYTES);
    const srd_t rx = mk_srd(ws, WS_END);
    const bool pre = (l == 0) && (w < 7);
    const unsigned wb = WS_WQF + ((bid >> 5) * 18 * 14 + 2 * w) * 1024;
    const unsigned evo = (unsigned)g * ACT_BYTES + t * 16, esb = WS_PART + l * 16 * ACT_BYTES + R * 9216 + w * 256, elb = (unsigned)(size_t)sm + w * 4096;
    const unsigned fpeer = (FL_C + l) * 2048 + (T * 16 + t) * 8;
    const int e4 = 36864 + (w >> 1) * (81920 - 36864 - 8192);

    u32x4 sel[2];
#pragma unroll
    for (int e = 0; e < 4; ++e) {
        sel[0][e] = (t == 2 * e) ? 0x00003f80u : ((t == 2 * e + 1) ? 0x3f800000u : 0u);
        sel[1][e] = (t == 8 + 2 * e) ? 0x00003f80u : ((t == 9 + 2 * e) ? 0x3f800000u : 0u);
    }
    asm volatile("" : "+v"(sel[0]), "+v"(sel[1]));
    if (pre) {
#pragma unroll
        for (int s = 0; s < E_N1; ++s)
#pragma unroll
            for (int j = 0; j < 2; ++j) we[s][j] = as_frag(ld16<SC1>(rws, lane * 16, wb + (s * 14 + j) * 1024));
    } else {
#pragma unroll
        for (int s = 0; s < E_N1; ++s) asm volatile("" : "=v"(we[s][0]), "=v"(we[s][1]));
    }
    CFENCE();
#define E_SUM(k, s0, s1) do { \
        bf16x8 v[8]; \
        _Pragma("unroll") for (int f = 0; f < ((k) < 2 ? 8 : 4); ++f) v[f] = lds_frag(sm + (f < 4 ? (s0) : (s1)) + w * 4096 + (f & 3) * 1024 + lane * 16); \
        SCHED(); \
        f32x4 a = {0.f, 0.f, 0.f, 0.f}; \
        _Pragma("unroll") for (int f = 0; f < ((k) < 2 ? 8 : 4); ++f) a = mfma16(as_frag(sel[f >> 2]), v[f], a); \
        a = a * ep.rsq + ep.r0[k]; \
        if ((k) < 2 || g < 2) st8(ro, ep.xo[k], 0, (u32x2){pack2(a[0], a[1]), pack2(a[2], a[3])}); \
    } while (0)
    if (w == 7) poll<true>(rws, fpeer, tag + 2, 0);
    else BAR();
    if (!EARLY || w == 7) { E_FETCH(0, C_SLOT(5)); E_FETCH(1, C_SLOT(6)); }
    E_FETCH(2, C_SLOT(7));
    E_FETCH(3, C_SLOT(8));
    if (w < 4) { const unsigned elb4 = elb + e4; _Pragma("unroll") for (int f = 0; f < 4; ++f) dma<SC1>(rx, evo, esb, f * 4 * (int)ACT_BYTES + 8192, elb4, f * 1024); }
    if (w < 4) WAIT_V(12); else WAIT_V(8);
    E_SUM(0, C_SLOT(5), C_SLOT(6));
    if (pre) {
#pragma unroll
        for (int s = E_N1; s < 18; ++s)
#pragma unroll
            for (int j = 0; j < 2; ++j) we[s][j] = as_frag(ld16<SC1>(rws, lane * 16, wb + (s * 14 + j) * 1024));
        WAIT_V(12);
    } else {
#pragma unroll
        for (int s = E_N1; s < 18; ++s) asm volatile("" : "=v"(we[s][0]), "=v"(we[s][1]));
        WAIT_V(0);
    }
    static_assert(2 * (18 - E_N1) == 12, "wait count");
    SCHED();
    E_SUM(1, C_SLOT(7), C_SLOT(8));
    if (w < 4) E_SUM(2, e4, 0);
    if (l == 0) publish(rws, FL_E * 2048 + R * 8, tag, w, lane);
#undef E_SUM
}

__global__ __launch_bounds__(512, 2) void fused_layers(const char* __restrict__ x0, const char* __restrict__ wqkv, const char* __restrict__ wo,
                                                       const char* __restrict__ wg, const char* __restrict__ wu, const char* __restrict__ wd,
                                                       char* __restrict__ out, char* __restrict__ ws) {
    extern __shared__ __attribute__((aligned(16))) char smem_[];
    lptr sm = (lptr)smem_;
    const int w = __builtin_amdgcn_readfirstlane(threadIdx.x >> 6);
    u64 tag = *(const __attribute__((address_space(4))) u64*)(ws + WS_GEN) + 0x9E3779B97F4A7C15ull;
    f32x4 acc[2][5];
    APre pre;
    a_gemm1(acc, pre, sm, ws, x0, wqkv, wo, w, tag);
#pragma unroll 1
    for (int l = 0;; ++l) {
        a_post(acc, pre, sm, ws, wd, l, w, tag);
        stage_b(sm, ws, l == 0 ? x0 : ws + WS_X2, l, w, tag);
        EPre ep;
        stage_c(ep, sm, ws, wg, wu, wd, l, w, tag);
        bf16x8 we[18][2];
        stage_e(we, ep, sm, ws, l == 0 ? ws + WS_X2 : out, l, w, tag);
        if (l) break;
        a_gemm2(acc, pre, we, sm, ws, w, tag);
    }
    if (blockIdx.x == 0 && w == 0) {
        const int lane = opaque_lane();
        const rsrc_t rws = mk_rsrc(ws, WS_END);
#pragma unroll
        for (int k = 0; k < 4; ++k) poll(rws, FL_A * 2048 + (k * 64 + lane) * 8, tag);
        if (lane == 0) st8(rws, 0, WS_GEN, __builtin_bit_cast(u32x2, tag));
    }
}

extern "C" void kernel_launch(void* const* d_in, const int* in_sizes, int n_in, void* d_out, int out_size, void* d_ws, size_t ws_size,
                              hipStream_t stream) {
    (void)in_sizes; (void)n_in; (void)out_size; (void)ws_size;
    (void)hipFuncSetAttribute((const void*)fused_layers, hipFuncAttributeMaxDynamicSharedMemorySize, 163840);
    fused_layers<<<256, 512, 163840, stream>>>((const char*)d_in[0], (const char*)d_in[1], (const char*)d_in[2], (const char*)d_in[4],
                                               (const char*)d_in[5], (const char*)d_in[6], (char*)d_out, (char*)d_ws);
}
```

```cpp
#define m5.cpp ../hip_kernels/v5a.cpp 1
#include <hip/hip_runtime.h>
#include <hip/hip_bf16.h>

#define LAS __attribute__((address_space(3)))
#define GAS __attribute__((address_space(1)))
typedef __bf16 bf16x8 __attribute__((ext_vector_type(8)));
typedef __bf16 bf16x4 __attribute__((ext_vector_type(4)));
typedef __bf16 bf16x2 __attribute__((ext_vector_type(2)));
typedef float f32x4 __attribute__((ext_vector_type(4)));
typedef float f32x2 __attribute__((ext_vector_type(2)));
typedef unsigned u32x4 __attribute__((ext_vector_type(4)));
typedef unsigned u32x2 __attribute__((ext_vector_type(2)));
typedef unsigned long long u64;
typedef LAS char* lptr;
typedef __amdgpu_buffer_rsrc_t rsrc_t;

#define DI __device__ __forceinline__
#define WAIT_V(n) asm volatile("s_waitcnt vmcnt(" #n ")" ::: "memory")
#define WAIT_V0() do { asm volatile("s_waitcnt vmcnt(0)" ::: "memory"); __builtin_amdgcn_s_waitcnt(0x0F70); asm volatile("" ::: "memory"); } while (0)
#define WAIT_L0() asm volatile("s_waitcnt lgkmcnt(0)" ::: "memory")
#define CFENCE() asm volatile("" ::: "memory")
#define BAR() do { CFENCE(); __builtin_amdgcn_s_barrier(); CFENCE(); } while (0)
#define SCHED() __builtin_amdgcn_sched_barrier(0)
#define SC1 16

constexpr int S_ = 2048, D_ = 576, F_ = 4608;
constexpr float LN_EPS = 1e-5f;
constexpr unsigned ACT_BYTES = (unsigned)S_ * D_ * 2;
constexpr unsigned WFF_BYTES = (unsigned)D_ * F_ * 2;
constexpr unsigned WQKV_BYTES = (unsigned)D_ * 3 * D_ * 2, WO_BYTES = (unsigned)D_ * D_ * 2;

constexpr unsigned WS_GEN = 0;
constexpr unsigned WS_FLAGS = 4096;
constexpr unsigned FL_A = 0, FL_B = 2, FL_C = 4, FL_E = 6, FL_S = 7;
constexpr unsigned WS_MEAN = 65536;
constexpr unsigned WS_STATS = 131072;
constexpr unsigned WS_WOF = 1u << 19;
constexpr unsigned WS_WQF = WS_WOF + 8 * 92160;
constexpr unsigned WS_O = WS_WQF + 8 * 258048;
constexpr unsigned WS_X1 = WS_O + ACT_BYTES;
constexpr unsigned WS_X2 = WS_X1 + ACT_BYTES;
constexpr unsigned WS_PART = WS_X2 + ACT_BYTES;
constexpr unsigned WS_END = WS_PART + 16 * ACT_BYTES;

DI rsrc_t mk_rsrc(const void* p, unsigned bytes) { return __builtin_amdgcn_make_buffer_rsrc((void*)p, 0, bytes, 0x00020000); }
typedef unsigned srd_t __attribute__((ext_vector_type(4)));
DI srd_t mk_srd(const void* p, unsigned bytes) {
    const u64 a = (u64)p;
    return (srd_t){(unsigned)a, (unsigned)(a >> 32) & 0xffffu, bytes, 0x00020000u};
}
template <int AUX = 0> DI void dma(srd_t r, unsigned voff, unsigned sb, int si, unsigned lb, int li) {
    unsigned tmp;
    if (AUX) asm volatile("s_add_i32 m0, %3, %5\n\ts_add_i32 %0, %4, %6\n\tbuffer_load_dwordx4 %1, %2, %0 offen sc1 lds"
                          : "=&s"(tmp) : "v"(voff), "s"(r), "s"(lb), "s"(sb), "i"(li), "i"(si) : "memory", "scc");
    else     asm volatile("s_add_i32 m0, %3, %5\n\ts_add_i32 %0, %4, %6\n\tbuffer_load_dwordx4 %1, %2, %0 offen lds"
                          : "=&s"(tmp) : "v"(voff), "s"(r), "s"(lb), "s"(sb), "i"(li), "i"(si) : "memory", "scc");
}
DI void dma_masked(srd_t r, unsigned voff, unsigned sb, int si, unsigned lb, int li, u64 mask) {
    unsigned tmp;
    asm volatile("s_add_i32 m0, %3, %5\n\ts_add_i32 %0, %4, %6\n\ts_mov_b64 exec, %7\n\tbuffer_load_dwordx4 %1, %2, %0 offen sc1 lds\n\ts_mov_b64 exec, -1"
                 : "=&s"(tmp) : "v"(voff), "s"(r), "s"(lb), "s"(sb), "i"(li), "i"(si), "s"(mask) : "memory", "scc");
}
DI void warm(srd_t r, unsigned voff, unsigned soff, unsigned lb) {
    asm volatile("s_mov_b32 m0, %0\n\ts_nop 0\n\tbuffer_load_dword %1, %2, %3 offen sc1 lds" :: "s"(lb), "v"(voff), "s"(r), "s"(soff) : "memory");
}
template <int AUX> DI u32x4 ld16(rsrc_t r, unsigned voff, unsigned soff) { return __builtin_amdgcn_raw_buffer_load_b128(r, voff, soff, AUX); }
template <int AUX> DI u32x2 ld8(rsrc_t r, unsigned voff, unsigned soff) { return __builtin_amdgcn_raw_buffer_load_b64(r, voff, soff, AUX); }
DI void st16(rsrc_t r, unsigned voff, unsigned soff, u32x4 v) {
    __builtin_amdgcn_raw_buffer_store_b128(v, r, voff, soff, SC1);
    asm volatile("s_nop 1" :: "v"(v) : "memory");
}
DI void st4(rsrc_t r, unsigned voff, unsigned soff, unsigned v) { __builtin_amdgcn_raw_buffer_store_b32(v, r, voff, soff, SC1); }
template <int AUX> DI unsigned ld4(rsrc_t r, unsigned voff, unsigned soff) { return __builtin_amdgcn_raw_buffer_load_b32(r, voff, soff, AUX); }
DI void st8(rsrc_t r, unsigned voff, unsigned soff, u32x2 v) { __builtin_amdgcn_raw_buffer_store_b64(v, r, voff, soff, SC1); }
DI bf16x8 lds_frag(lptr p) { return *(const LAS bf16x8*)p; }
DI bf16x8 lds_tr2(lptr p0, lptr p1) {
    bf16x4 lo = __builtin_amdgcn_ds_read_tr16_b64_v4bf16((LAS bf16x4*)p0);
    bf16x4 hi = __builtin_amdgcn_ds_read_tr16_b64_v4bf16((LAS bf16x4*)p1);
    return __builtin_shufflevector(lo, hi, 0, 1, 2, 3, 4, 5, 6, 7);
}
DI f32x4 mfma16(bf16x8 a, bf16x8 b, f32x4 c) { return __builtin_amdgcn_mfma_f32_16x16x32_bf16(a, b, c, 0, 0, 0); }
DI void mfma_acc(f32x4& acc, bf16x8 a, bf16x8 b) { asm volatile("v_mfma_f32_16x16x32_bf16 %0, %1, %2, %0" : "+v"(acc) : "v"(a), "v"(b)); }
DI float shfl(float v, int src) { return __int_as_float(__builtin_amdgcn_ds_bpermute(src << 2, __float_as_int(v))); }
DI bf16x8 as_frag(u32x4 v) { return __builtin_bit_cast(bf16x8, v); }
DI unsigned pack2(float a, float b) {
    f32x2 f = {a, b};
    bf16x2 h = __builtin_convertvector(f, bf16x2);
    return __builtin_bit_cast(unsigned, h);
}
DI float bf_lo(unsigned u) { return __uint_as_float(u << 16); }
DI float bf_hi(unsigned u) { return __uint_as_float(u & 0xffff0000u); }
DI float dot2(unsigned a, unsigned b, float c) {
    return __builtin_amdgcn_fdot2_f32_bf16(__builtin_bit_cast(bf16x2, a), __builtin_bit_cast(bf16x2, b), c, false);
}
DI float fmax_raw(float a, float b) { float r; asm("v_max_f32 %0, %1, %2" : "=v"(r) : "v"(a), "v"(b)); return r; }
DI float fmax3_raw(float a, float b, float c) { float r; asm("v_max3_f32 %0, %1, %2, %3" : "=v"(r) : "v"(a), "v"(b), "v"(c)); return r; }
DI float relu(float x) { const int i = __float_as_int(x); return __int_as_float(i > 0 ? i : 0); }
DI float reglu(float g, float u) { float p = relu(g) * u; asm("" : "+v"(p)); return p; }
DI float red_sum_g(float v) {
    auto a = __builtin_amdgcn_permlane16_swap(__float_as_uint(v), __float_as_uint(v), false, false);
    v = __uint_as_float(a[0]) + __uint_as_float(a[1]);
    auto b = __builtin_amdgcn_permlane32_swap(__float_as_uint(v), __float_as_uint(v), false, false);
    return __uint_as_float(b[0]) + __uint_as_float(b[1]);
}
DI float red_max_g(float v) {
    auto a = __builtin_amdgcn_permlane16_swap(__float_as_uint(v), __float_as_uint(v), false, false);
    v = fmax_raw(__uint_as_float(a[0]), __uint_as_float(a[1]));
    auto b = __builtin_amdgcn_permlane32_swap(__float_as_uint(v), __float_as_uint(v), false, false);
    return fmax_raw(__uint_as_float(b[0]), __uint_as_float(b[1]));
}
DI int opaque_lane() { int l; asm volatile("v_mbcnt_lo_u32_b32 %0, -1, 0\n\tv_mbcnt_hi_u32_b32 %0, -1, %0" : "=v"(l)); return l; }
DI int opaque_s(int v) { asm volatile("" : "+s"(v)); return v; }

DI float bcast_row0(float v) { return __int_as_float(__builtin_amdgcn_update_dpp(0, __float_as_int(v), 0x150, 0xf, 0xf, false)); }

DI void set_flag(rsrc_t rws, unsigned off, u64 val, int lane) {
    if (lane == 0) st8(rws, 0, WS_FLAGS + off, __builtin_bit_cast(u32x2, val));
    CFENCE();
}
DI void publish(rsrc_t rws, unsigned off, u64 val, int w, int lane) {
    WAIT_V0();
    BAR();
    if (w == 0) set_flag(rws, off, val, lane);
}
DI u64 flag_ld(rsrc_t rws, unsigned off) { CFENCE(); const u64 v = __builtin_bit_cast(u64, ld8<SC1>(rws, off, WS_FLAGS)); CFENCE(); return v; }
template <bool WITH_BAR = false> DI void poll(rsrc_t rws, unsigned off, u64 lo, unsigned span = 0) {
    u64 v0, v1, v2 = 0;
    v0 = flag_ld(rws, off); __builtin_amdgcn_s_sleep(4);
    v1 = flag_ld(rws, off); __builtin_amdgcn_s_sleep(4);
#pragma unroll 1
    for (unsigned it = 0; it < (1u << 19); ++it) {
        v2 = flag_ld(rws, off); if (__all(v0 - lo <= span)) break;
        v0 = flag_ld(rws, off); if (__all(v1 - lo <= span)) break;
        v1 = flag_ld(rws, off); if (__all(v2 - lo <= span)) break;
    }
    if (WITH_BAR) BAR();
    asm volatile("" :: "v"(v0), "v"(v1), "v"(v2) : "memory");
}

constexpr int A_SCR = 92160, A_SCR_RS = 1168;
constexpr int A_SCRAP = 160768;
constexpr int A_QKV = 51200, A_RS = 464;
constexpr int A_ST = 162816;
constexpr int A_MEAN = 163456;
constexpr int B_W1 = 92160, B_W2 = 0, B_N1 = 65;
constexpr int B_RED = A_QKV;
constexpr unsigned ONES2 = 0x3f803f80u;
constexpr int E_N1 = 12;
constexpr unsigned FL_Q = 8 * 2048;
#define WOSP 2
#define WARMC 1
#define WARMF 2

struct ACtx { int lane, w, tt, hg; lptr ta0, ta1; unsigned xo, xs[4], xl[4]; u64 xm; };
DI ACtx a_ctx(lptr sm, const int w_) {
    ACtx c;
    c.lane = opaque_lane();
    const int bid = opaque_s(blockIdx.x);
    c.w = opaque_s(w_);
    c.tt = (bid & 7) * 4 + ((bid >> 3) & 3); c.hg = bid >> 5;
    const int t = c.lane & 15, g = c.lane >> 4;
    c.ta0 = sm + t * 128 + ((g ^ (t & 7)) * 16); c.ta1 = sm + t * 128 + (((4 + g) ^ (t & 7)) * 16);
    c.xo = (unsigned)((c.lane >> 3) * 1152 + ((c.lane & 7) ^ (c.lane >> 3)) * 16);
#pragma unroll
    for (int i = 0; i < 4; ++i) {
        int id = i * 8 + c.w - 3; id = id > 26 ? 26 : id;
        int kq = (id >= 18) ? 2 : (id >= 9 ? 1 : 0), rb = 1 + id - kq * 9;
        if (id < 0) { kq = c.w; rb = 0; }
        int r0 = c.tt * 64 - 16 + rb * 8; r0 = r0 < 0 ? r0 + 16 : r0;
        c.xs[i] = (unsigned)(r0 * 1152 + kq * 128);
        c.xl[i] = (unsigned)(size_t)sm + (kq * 10 + rb) * 1024;
    }
    c.xm = c.w < 3 ? ~0xffull : ~0ull;
    return c;
}
DI void a_xdma(const ACtx& c, const srd_t rx, const int third) {
    dma_masked(rx, c.xo, c.xs[0], third * 384, c.xl[0], third * 30720, c.xm);
#pragma unroll
    for (int i = 1; i < 4; ++i) dma<SC1>(rx, c.xo, c.xs[i], third * 384, c.xl[i], third * 30720);
}
DI bf16x8 a_ones_frag() {
    u32x4 o;
#pragma unroll
    for (int e = 0; e < 4; ++e) asm volatile("v_mov_b32 %0, 0x3f803f80" : "=v"(o[e]));
    return as_frag(o);
}
DI void a_ones(lptr sm, const int lane) {
    const bf16x8 o = a_ones_frag();
    if (lane < 8) {
#pragma unroll
        for (int kb = 0; kb < 9; ++kb) *(LAS bf16x8*)(sm + kb * 10240 + lane * 16) = o;
    }
}
DI void a_tok(bf16x8 (&tk)[5], const ACtx& c, const int s) {
    lptr ta = ((s & 1) ? c.ta1 : c.ta0) + (s >> 1) * 10240;
#pragma unroll
    for (int i = 0; i < 5; ++i) tk[i] = lds_frag(ta + i * 2048);
}
template <bool STATS> DI void a_mm(f32x4 (&acc)[2][5], const bf16x8 (&tk)[5], const bf16x8 f0, const bf16x8 f1, const int s, const int i0, const int i1,
                                   bf16x8 (&tn)[5], const ACtx& c) {
    const f32x4 z = {0.f, 0.f, 0.f, 0.f};
    lptr ta = (((s + 1) & 1) ? c.ta1 : c.ta0) + ((s + 1) >> 1) * 10240;
#pragma unroll
    for (int i = i0; i < i1; ++i) {
        SCHED();
        if (s + 1 < 18) tn[i] = lds_frag(ta + i * 2048);
        SCHED();
        const bf16x8 g1 = STATS ? tk[i] : f1;
        if (s == 0) { acc[0][i] = mfma16(f0, tk[i], z); acc[1][i] = mfma16(g1, tk[i], z); }
        else if (s == 17) { acc[0][i] = mfma16(f0, tk[i], acc[0][i]); acc[1][i] = mfma16(g1, tk[i], acc[1][i]); }
        else { mfma_acc(acc[0][i], f0, tk[i]); mfma_acc(acc[1][i], g1, tk[i]); }
    }
}
DI void a_we(bf16x8 (&we)[18][2], char* ws, const int lane, const int hg, const int w, const bool on, const int s0, const int s1) {
    const rsrc_t r = mk_rsrc(ws + WS_WQF + (hg * 18 * 14 + 2 * (w < 7 ? w : 0)) * 1024, on ? 258048u : 0u);
#pragma unroll
    for (int s = s0; s < s1; ++s)
#pragma unroll
        for (int j = 0; j < 2; ++j) we[s][j] = as_frag(ld16<SC1>(r, lane * 16, (s * 14 + j) * 1024));
    CFENCE();
}
DI void a_stats_out(const f32x4 (&acc)[2][5], lptr sm, const int lane) {
    const int t = lane & 15, g = lane >> 4;
    if (g == (t >> 2)) {
#pragma unroll
        for (int i = 0; i < 5; ++i) {
            const float sq = (t & 2) ? ((t & 1) ? acc[1][i][3] : acc[1][i][2]) : ((t & 1) ? acc[1][i][1] : acc[1][i][0]);
            const float mean = acc[0][i][0] * (1.0f / D_);
            const float rstd = __builtin_amdgcn_rsqf(fmaxf(sq * (1.0f / D_) - mean * mean, 0.0f) + LN_EPS);
            *(LAS f32x2*)(sm + A_ST + (16 * i + t) * 8) = (f32x2){mean * rstd, rstd};
            *(LAS float*)(sm + A_MEAN + (16 * i + t) * 4) = mean;
        }
    }
}
struct APre { f32x4 bias[2]; unsigned ow, ost, rb, vb, oo[3]; };
DI void a_pre(APre& p, const ACtx& c) {
    const int lane = c.lane, t = lane & 15, g = lane >> 4, i = c.w >> 1, hp = c.w & 1;
    const int pos = c.tt * 64 + 16 * i + t;
    const unsigned V = 0x5555u & ((2u << (pos < 14 ? pos : 14)) - 1u);
    const int sh = 13 + t - 4 * g;
    const unsigned U[2] = {~(V >> sh), ~((V << 16) >> sh)};
#pragma unroll
    for (int a = 0; a < 2; ++a)
#pragma unroll
        for (int r = 0; r < 4; ++r) p.bias[a][r] = __uint_as_float((unsigned)__builtin_amdgcn_sbfe((int)U[a], 3 - r, 1) & 0xf149f2cau);
    p.ow = (unsigned)(A_QKV + t * A_RS + c.w * 64 + g * 8);
    p.ost = (unsigned)(A_ST + t * 8);
    p.rb = (unsigned)(A_QKV + (16 * i + t) * A_RS + hp * 72 + g * 16);
    p.vb = (unsigned)(A_QKV + (16 * i + (lane >> 2)) * A_RS + 288 + hp * 72 + (lane & 3) * 8);
#pragma unroll
    for (int k = 0; k < 3; ++k) {
        const int d = c.hg * 72 + hp * 36 + 16 * k + 4 * g;
        p.oo[k] = (unsigned)((d >> 5) * 131072 + ((d >> 3) & 3) * 256 + t * 16 + (d & 7) * 2);
    }
    asm volatile("" : "+v"(p.bias[0]), "+v"(p.bias[1]), "+v"(p.ow), "+v"(p.ost), "+v"(p.rb), "+v"(p.vb), "+v"(p.oo[0]), "+v"(p.oo[1]), "+v"(p.oo[2]));
}

DI void ffn_warm(lptr sm, const char* wg_, const char* wu_, const char* wd_, const int lane, const int bid, const int k) {
    const int lq = lane < 54 ? lane : 53;
    const unsigned scrap = (unsigned)(size_t)sm + A_SCRAP;
    if (k < 6) warm(mk_srd((k & 1) ? wu_ : wg_, WFF_BYTES), (unsigned)((lq / 9) * 9216 + (lq % 9) * 128), (unsigned)(((bid >> 3) * 18 + (k >> 1) * 6) * 9216 + (bid & 7) * 1152), scrap);
    else warm(mk_srd(wd_, WFF_BYTES), lq * 128, (unsigned)((bid & 7) * 663552 + (bid >> 3) * 20736 + (k - 6) * 6912), scrap);
}

DI void stage_0(bf16x8 (&we)[18][2], lptr sm, char* ws, const char* xin, const char* wqkv_, const char* wo_,
                const char* wg_, const char* wu_, const char* wd_, const int w_, u64& tag) {
    const int lane = opaque_lane(), w = w_;
    const int bid = blockIdx.x;
    const int tt = (bid & 7) * 4 + ((bid >> 3) & 3), hg = bid >> 5;
    const rsrc_t rws = mk_rsrc(ws, WS_END);
    const unsigned lw = (unsigned)((lane >> 3) * A_SCR_RS + (lane & 7) * 16), lr = (unsigned)(((lane >> 2) & 3) * A_SCR_RS);
    if (w < 7 && tt < 18) {
        const srd_t rw = mk_srd(wqkv_, WQKV_BYTES);
        const unsigned dst = (unsigned)(size_t)sm + A_SCR + w * 2048;
#pragma unroll
        for (int i = 0; i < 2; ++i) {
            const int p = i * 16 + (lane >> 2);
            int cc = 4 * w + ((lane & 3) ^ (((p >> 3) & 1) << 1));
            cc = cc > 26 ? 26 : cc;
            const int mat = (cc >= 18) ? 2 : (cc >= 9 ? 1 : 0);
            dma(rw, (unsigned)(p * 3456 + mat * (1152 - 144) + cc * 16), (unsigned)(tt * 32 * 3456 + hg * 144), 0, dst, i * 1024);
        }
    }
    if (w == 7 && WARMC) {
        u64 pc; unsigned len;
        asm volatile("s_getpc_b64 %0\n.Lfl_code_begin:\n\ts_mov_b32 %1, .Lfl_code_end-.Lfl_code_begin" : "=s"(pc), "=s"(len));
        const unsigned o = lane * 128 + (unsigned)((bid >> 3) & 7) * 8192;
        if ((int)len > 64) warm(mk_srd((const void*)pc, len), o < len - 4 ? o : 0u, 0u, (unsigned)(size_t)sm + A_SCRAP);
    }
    if (w == 6 && tt >= 23) {
        const int rb = (tt - 23) * 8 + hg, g = lane >> 4, pp = lane & 3;
        const rsrc_t rr = mk_rsrc(wo_, WO_BYTES);
        lptr scr = sm + A_SCR + 14336;
        u32x4 v[9];
#pragma unroll
        for (int j = 0; j < 9; ++j) v[j] = ld16<0>(rr, (lane >> 3) * 1152 + (lane & 7) * 16, rb * 9216 + j * 128);
        float s0 = 0.f, s1 = 0.f;
#pragma unroll
        for (int j = 0; j < 9; ++j) {
            *(LAS u32x4*)(scr + lw + j * 128) = v[j];
            s0 = dot2(v[j][0], ONES2, s0); s1 = dot2(v[j][1], ONES2, s1); s0 = dot2(v[j][2], ONES2, s0); s1 = dot2(v[j][3], ONES2, s1);
        }
        float s = s0 + s1;
        s += shfl(s, lane ^ 1); s += shfl(s, lane ^ 2); s += shfl(s, lane ^ 4);
        s *= (1.0f / D_);
        const float hi = bf_lo(pack2(s, 0.f));
        if ((lane & 7) == 0) *(LAS u32x2*)(scr + lw + 1152) = (u32x2){pack2(hi, s - hi), 0u};
        WAIT_L0();
#pragma unroll 1
        for (int k = 0; k < 10; ++k) {
            const int f = 4 * k + g, cb = f / 5, j = f - cb * 5;
            lptr p = scr + lr + ((j == 4 && pp >= 2) ? 1152 : cb * 144 + j * 32 + pp * 8);
            const bf16x8 fr = lds_tr2(p, p + 4 * A_SCR_RS);
            st16(rws, (unsigned)(cb * 92160 + j * 1024 + (lane & 15) * 16), WS_WOF + (rb >> 2) * 5120 + (rb & 3) * 256, __builtin_bit_cast(u32x4, fr));
        }
        WAIT_V(0);
        set_flag(rws, FL_S * 2048 + rb * 8, tag, lane);
    }
    asm volatile("" : "+s"(tag));
    const ACtx c = a_ctx(sm, w);
    const srd_t rx = mk_srd(xin, ACT_BYTES);
    if (hg < 3) { a_xdma(c, rx, 0); a_xdma(c, rx, 1); a_xdma(c, rx, 2); }
    else if (hg < 6) { a_xdma(c, rx, 1); a_xdma(c, rx, 2); a_xdma(c, rx, 0); }
    else { a_xdma(c, rx, 2); a_xdma(c, rx, 0); a_xdma(c, rx, 1); }
    if (w == 7) { a_ones(sm, lane); WAIT_L0(); }
    if (w < 7 && tt < 18) {
        const int g = lane >> 4;
        lptr p0 = sm + A_SCR + w * 2048 + (8 * g + ((lane >> 2) & 3)) * 64 + ((g & 1) * 32) + (lane & 3) * 8;
        lptr p1 = p0 + ((g & 1) ? -32 : 32);
        WAIT_V(12);
        const bf16x8 f0 = lds_tr2(p0, p0 + 256), f1 = lds_tr2(p1, p1 + 256);
        const unsigned o = WS_WQF + ((hg * 18 + tt) * 14 + 2 * w) * 1024;
        st16(rws, lane * 16, o, __builtin_bit_cast(u32x4, f0));
        st16(rws, lane * 16, o + 1024, __builtin_bit_cast(u32x4, f1));
    }
    if (tt < 18) {
        WAIT_V(0);
        BAR();
        if (w == 0) set_flag(rws, FL_Q + hg * 256 + tt * 8, tag, lane);
    }
    WAIT_V0();
    if (w == 7) poll<true>(rws, FL_Q + hg * 256 + (lane < 18 ? lane : 0) * 8, tag);
    else BAR();
    a_we(we, ws, lane, hg, w, w < 7, 0, E_N1);
    (void)wg_; (void)wu_; (void)wd_;
}

DI void a_gemm(f32x4 (&acc)[2][5], APre& pre, bf16x8 (&we)[18][2], lptr sm, char* ws, const char* wg_, const char* wu_, const char* wd_,
               const int l_, const int w_, u64 tag) {
    asm volatile("" : "+s"(tag));
    const ACtx c = a_ctx(sm, w_);
    const int lane = c.lane, w = c.w, l = opaque_s(l_), bid = opaque_s(blockIdx.x);
    const srd_t rx = mk_srd(ws + WS_X2, ACT_BYTES), rs = mk_srd(ws, WS_END);
    const rsrc_t rws = mk_rsrc(ws, WS_END);
    const unsigned l16 = lane * 16, wsb = WS_WOF + c.hg * 92160 + w * 1024, wlb = (unsigned)(size_t)sm + w * 1024;
    a_we(we, ws, lane, c.hg, w, w < 7, E_N1, 18);
    WAIT_V0();
    if (l) {
        if (w == 7) {
            a_ones(sm, lane);
            int i = 8 * c.tt - 4 + (lane < 12 ? lane : 11); i = i < 0 ? 0 : i;
            WAIT_L0();
            poll<true>(rws, FL_E * 2048 + i * 8, tag);
        } else {
            SCHED();
            BAR();
        }
        a_xdma(c, rx, 0); a_xdma(c, rx, 1);
        WAIT_V(4); BAR();
    } else if (w == 7) {
        poll(rws, FL_S * 2048 + lane * 8, tag);
        poll(rws, FL_S * 2048 + (64 + (lane & 7)) * 8, tag);
    }
    bf16x8 tk[2][5];
#define B_WD(k) do { if ((k) < 8) dma<SC1>(rs, l16, wsb, (k) * 8192, wlb, B_W1 + (k) * 8192); \
                     else if ((k) < 11) dma<SC1>(rs, l16, wsb, (B_N1 + ((k) - 8) * 8) * 1024, wlb, B_W2 + ((k) - 8) * 8192); \
                     else if (w == 0) { dma<SC1>(rs, l16, wsb, 65536, wlb, B_W1 + 65536); dma<SC1>(rs, l16, wsb, 89 * 1024, wlb, B_W2 + 24576); } } while (0)
#define A_LOOP(MM) \
    a_tok(tk[0], c, 0); \
    _Pragma("unroll") for (int s = 0; s < 18; ++s) { \
        if (s == 5) { WAIT_V(0); WAIT_L0(); BAR(); }     \
        if (s == 11) { } \
        MM(0, 2); \
        SCHED(); \
        if (s == 0) { if (l) a_xdma(c, rx, 2); } \
        if (WOSP == 1) { if (s >= 5 && s < 17) B_WD(s - 5); } \
        else { if (s >= 5 && s < 11) { B_WD(2 * (s - 5)); B_WD(2 * (s - 5) + 1); } } \
        if (s == (WOSP == 1 ? 17 : 11)) { if (WARMF && l == 0 && w < 6) ffn_warm(sm, wg_, wu_, wd_, lane, bid, w); } \
        if (s == 16) a_pre(pre, c); \
        MM(2, 5); \
        SCHED(); \
    }
#define A_MS(i0, i1) a_mm<true>(acc, tk[s & 1], ones, ones, s, i0, i1, tk[(s + 1) & 1], c)
#define A_MN(i0, i1) a_mm<false>(acc, tk[s & 1], we[s][0], we[s][1], s, i0, i1, tk[(s + 1) & 1], c)
    if (w == 7) {
        const bf16x8 ones = a_ones_frag();
        __builtin_amdgcn_s_setprio(1);
        A_LOOP(A_MS)
        a_stats_out(acc, sm, lane);
        __builtin_amdgcn_s_setprio(0);
    } else {
        A_LOOP(A_MN)
    }
#undef A_LOOP
#undef B_WD
#undef A_MS
#undef A_MN
    if (l) { } else { }
}

DI void a_post(f32x4 (&acc)[2][5], const APre& pre, lptr sm, char* ws, const int l_, const int w_, u64 tag) {
    asm volatile("" : "+s"(tag));
    const int lane = opaque_lane();
    const int bid = opaque_s(blockIdx.x), w = opaque_s(w_), l = opaque_s(l_);
    const int tt = (bid & 7) * 4 + ((bid >> 3) & 3), hg = bid >> 5;
    const int g = lane >> 4;
    const rsrc_t rws = mk_rsrc(ws, WS_END);
    f32x2 csb[2][2];
#pragma unroll
    for (int j = 0; j < 2; ++j)
#pragma unroll
        for (int r = 0; r < 4; ++r) csb[j][r >> 1][r & 1] = bcast_row0(acc[j][0][r]);
    WAIT_L0();
    SCHED();
    BAR();
    if (w < 7) {
        f32x2 st[5];
#pragma unroll
        for (int i = 0; i < 5; ++i) st[i] = *(const LAS f32x2*)(sm + pre.ost + 128 * i);
        SCHED();
#pragma unroll
        for (int i = 0; i < 5; ++i) {
#pragma unroll
            for (int j = 0; j < 2; ++j) {
                const f32x2 a = (f32x2){acc[j][i][0], acc[j][i][1]} * st[i][1] - csb[j][0] * st[i][0];
                const f32x2 b = (f32x2){acc[j][i][2], acc[j][i][3]} * st[i][1] - csb[j][1] * st[i][0];
                *(LAS u32x2*)(sm + pre.ow + 16 * i * A_RS + j * 32) = (u32x2){pack2(a[0], a[1]), pack2(b[0], b[1])};
            }
        }
    }
    WAIT_L0();
    BAR();
    {
        lptr rb = sm + pre.rb;
        u32x4 qf[2]; bf16x8 kf[2][2], vf[3];
#pragma unroll
        for (int hh = 0; hh < 2; ++hh) {
            const u32x2 a = *(const LAS u32x2*)(rb + 16 * A_RS + hh * 8), b = *(const LAS u32x2*)(rb + 16 * A_RS + hh * 8 + 8);
            qf[hh] = (u32x4){a[0], a[1], b[0], b[1]};
#pragma unroll
            for (int k = 0; k < 2; ++k) {
                const u32x2 c = *(const LAS u32x2*)(rb + 144 + 16 * k * A_RS + hh * 8), d = *(const LAS u32x2*)(rb + 144 + 16 * k * A_RS + hh * 8 + 8);
                kf[hh][k] = as_frag((u32x4){c[0], c[1], d[0], d[1]});
            }
        }
        lptr vb = sm + pre.vb;
#pragma unroll
        for (int c = 0; c < 3; ++c) vf[c] = lds_tr2(vb + 32 * c, vb + 32 * c + 16 * A_RS);
        SCHED();
        qf[0][0] = g < 3 ? qf[0][0] : 0u; qf[0][1] = g < 2 ? qf[0][1] : 0u; qf[0][2] = g < 2 ? qf[0][2] : 0u; qf[0][3] = g < 2 ? qf[0][3] : 0u;
        qf[1][0] = g > 1 ? qf[1][0] : 0u; qf[1][1] = g > 1 ? qf[1][1] : 0u; qf[1][2] = g > 1 ? qf[1][2] : 0u; qf[1][3] = g > 0 ? qf[1][3] : 0u;
        f32x4 sc[2][2];
#pragma unroll
        for (int hh = 0; hh < 2; ++hh)
#pragma unroll
            for (int k = 0; k < 2; ++k) sc[hh][k] = mfma16(kf[hh][k], as_frag(qf[hh]), pre.bias[k]);
        constexpr float kScale = 0.23570226039551584f * 1.4426950408889634f;
        float mx[2], sum[2];
#pragma unroll
        for (int hh = 0; hh < 2; ++hh) {
            const float m0 = fmaxf(fmaxf(sc[hh][0][0], sc[hh][0][1]), fmaxf(sc[hh][0][2], sc[hh][0][3]));
            const float m1 = fmaxf(fmaxf(sc[hh][1][0], sc[hh][1][1]), fmaxf(sc[hh][1][2], sc[hh][1][3]));
            mx[hh] = fmaxf(m0, m1);
        }
        mx[0] = red_max_g(mx[0]) * -kScale; mx[1] = red_max_g(mx[1]) * -kScale;
#pragma unroll
        for (int hh = 0; hh < 2; ++hh) {
#pragma unroll
            for (int k = 0; k < 2; ++k)
#pragma unroll
                for (int r = 0; r < 4; ++r) sc[hh][k][r] = __builtin_amdgcn_exp2f(__builtin_fmaf(sc[hh][k][r], kScale, mx[hh]));
            const f32x4 s4 = sc[hh][0] + sc[hh][1];
            sum[hh] = (s4[0] + s4[1]) + (s4[2] + s4[3]);
        }
        sum[0] = __builtin_amdgcn_rcpf(red_sum_g(sum[0])); sum[1] = __builtin_amdgcn_rcpf(red_sum_g(sum[1]));
        f32x4 o[2][2];
#pragma unroll
        for (int hh = 0; hh < 2; ++hh) {
            const f32x4 p0 = sc[hh][0] * sum[hh], p1 = sc[hh][1] * sum[hh];
            const u32x4 pf = {pack2(p0[0], p0[1]), pack2(p0[2], p0[3]), pack2(p1[0], p1[1]), pack2(p1[2], p1[3])};
            o[hh][0] = mfma16(vf[hh], as_frag(pf), (f32x4){0.f, 0.f, 0.f, 0.f});
            o[hh][1] = mfma16(vf[hh + 1], as_frag(pf), (f32x4){0.f, 0.f, 0.f, 0.f});
        }
        const unsigned ob = WS_O + (tt * 4 + (w >> 1)) * 1024;
        st8(rws, pre.oo[0], ob, (u32x2){pack2(o[0][0][0], o[0][0][1]), pack2(o[0][0][2], o[0][0][3])});
        const unsigned e = pack2(o[0][1][0], o[0][1][1]), f = pack2(o[1][0][0], o[1][0][1]);
        st8(rws, pre.oo[1], ob, (u32x2){g == 0 ? e : f, pack2(o[1][0][2], o[1][0][3])});
        if (g == 0) st8(rws, pre.oo[2], ob, (u32x2){pack2(o[1][1][0], o[1][1][1]), pack2(o[1][1][2], o[1][1][3])});
    }
    publish(rws, (FL_A + l) * 2048 + (tt * 8 + hg) * 8, tag, w, lane);
}

DI void stage_b(lptr sm, char* ws, const char* xin, const int l_, const int w_, u64 tag) {
    asm volatile("" : "+s"(tag));
    const int lane = opaque_lane();
    const int bid = opaque_s(blockIdx.x), w = opaque_s(w_), l = opaque_s(l_);
    const int tt = (bid & 7) * 4 + ((bid >> 3) & 3), cb = bid >> 5;
    const int t = lane & 15, g = lane >> 4;
    const int ti = w & 3, kh = w >> 2;
    const int token = tt * 64 + ti * 16 + t;
    const rsrc_t rws = mk_rsrc(ws, WS_END), rx = mk_rsrc(xin, ACT_BYTES);
    const unsigned l16 = lane * 16;

    u32x2 res[3];
    {
        const unsigned o = (unsigned)(token * 1152 + g * 8);
        res[0] = ld8<SC1>(rx, o, cb * 144 + kh * 64);
        res[1] = ld8<SC1>(rx, o + 32, cb * 144 + kh * 64);
        res[2] = ld8<SC1>(rx, o + ((g < 2) ? 128 : 0), cb * 144);
    }
    bf16x8 wf[9][5];
    {
        lptr ba = sm + B_W1 + kh * 46080 + l16, bb = sm + (kh ? B_W2 : B_W1 + 20480) + l16;
#pragma unroll
        for (int s = 0; s < 9; ++s)
#pragma unroll
            for (int j = 0; j < 5; ++j) wf[s][j] = lds_frag(s < 4 ? ba + (s * 5 + j) * 1024 : bb + ((s - 4) * 5 + j) * 1024);
    }
    f32x4 r0[3]; unsigned xo[3];
    float xmean = *(const LAS float*)(sm + A_MEAN + (16 + ti * 16 + t) * 4);
#pragma unroll
    for (int j = 0; j < 3; ++j) {
        r0[j] = (f32x4){bf_lo(res[j][0]), bf_hi(res[j][0]), bf_lo(res[j][1]), bf_hi(res[j][1])} - xmean;
        const int d = cb * 72 + (j < 2 ? 32 * kh + 16 * j : 64) + 4 * g;
        xo[j] = (unsigned)((d >> 5) * 131072 + ((d >> 3) & 3) * 256 + t * 16 + (d & 7) * 2);
        asm volatile("" : "+v"(r0[j]), "+v"(xo[j]));
    }
    unsigned so = (unsigned)(token * 64), bsrc = (unsigned)((32 + t) * 4);
    asm volatile("" : "+v"(so), "+v"(bsrc), "+v"(xmean));
    SCHED();
    if (w == 7) poll<true>(rws, (FL_A + l) * 2048 + (tt * 8 + (lane & 7)) * 8, tag);
    else BAR();
    bf16x8 of[9];
    const unsigned ob = WS_O + (kh * 9 * 128 + tt * 4 + ti) * 1024;
#pragma unroll
    for (int s = 0; s < 9; ++s) of[s] = as_frag(ld16<SC1>(rws, l16, ob + s * 131072));
    CFENCE(); SCHED();
    f32x4 acc[5];
#pragma unroll
    for (int s = 0; s < 9; ++s) {
#pragma unroll
        for (int j = 0; j < 5; ++j) acc[j] = mfma16(wf[s][j], of[s], s ? acc[j] : (f32x4){0.f, 0.f, 0.f, 0.f});
        SCHED();
    }
    f32x4 own[3];
    {
        lptr rd = sm + B_RED + w * 3072 + l16;
        if (kh) { *(LAS f32x4*)(rd) = acc[0]; *(LAS f32x4*)(rd + 1024) = acc[1]; own[0] = acc[2] + r0[0]; own[1] = acc[3] + r0[1]; }
        else    { *(LAS f32x4*)(rd) = acc[2]; *(LAS f32x4*)(rd + 1024) = acc[3]; own[0] = acc[0] + r0[0]; own[1] = acc[1] + r0[1]; }
        *(LAS f32x4*)(rd + 2048) = acc[4];
        own[2] = acc[4];
    }
    WAIT_L0();
    BAR();
    {
        lptr rd = sm + B_RED + (w ^ 4) * 3072 + l16;
        const f32x4 p2 = *(const LAS f32x4*)(rd + 2048), p0 = *(const LAS f32x4*)(rd), p1 = *(const LAS f32x4*)(rd + 1024);
        own[2] += p2;
        const float dm = __int_as_float(__builtin_amdgcn_ds_bpermute(bsrc, __float_as_int(own[2][0] + own[2][1])));
        own[0] += p0; own[1] += p1; own[2] += r0[2];
        float sq = 0.f;
        const unsigned xb = WS_X1 + (tt * 4 + ti) * 1024;
#pragma unroll
        for (int j = 0; j < 3; ++j) {
            const f32x4 v = own[j] - dm;
            const u32x2 pk = {pack2(v[0], v[1]), pack2(v[2], v[3])};
            if (j < 2 || (g < 2 && kh == 0)) {
                st8(rws, xo[j], xb, pk);
                sq = dot2(pk[0], pk[0], sq); sq = dot2(pk[1], pk[1], sq);
            }
        }
        sq = red_sum_g(sq);
        if (g == 0) st4(rws, so, WS_STATS + cb * 8 + kh * 4, __float_as_uint(sq));
        if (cb == 0 && kh == 0) { if (g == 1) st4(rws, so >> 4, WS_MEAN, __float_as_uint(xmean + dm)); }
    }
    WAIT_L0();
    publish(rws, (FL_B + l) * 2048 + (tt * 8 + cb) * 8, tag, w, lane);
}

#define EARLY 0
#define WARM1 0
#define WARM2 0
constexpr int C_BUF = 45056;
constexpr int C_S2 = 90112, C_S3 = 126976;
#define C_SLOT(b) (((b) & 1) ? (((b) & 2) ? C_S3 : C_S2) : (((b) & 2) ? C_BUF : 0))
#define E_FETCH(k, slot) do { _Pragma("unroll") for (int f_ = 0; f_ < 4; ++f_) \
        dma<SC1>(rx, evo, esb, f_ * 4 * (int)ACT_BYTES + (k) * 2048, elb, (slot) + f_ * 1024); } while (0)

struct EPre { float rsq; f32x4 r0[3]; unsigned xo[3]; };

DI void stage_c(EPre& ep, lptr sm, char* ws, const char* wg_, const char* wu_, const char* wd_, const int l_, const int w_, u64 tag) {
    asm volatile("" : "+s"(tag));
    const int lane = opaque_lane();
    const int bid = opaque_s(blockIdx.x), w = opaque_s(w_), l = opaque_s(l_);
    const int fs = 2 * (bid & 7) + ((bid >> 3) & 1), T = bid >> 4;
    const int m = w >> 2, n = w & 3;
    const int g = lane >> 4;
    const int q = (lane >> 2) & 3, pp = lane & 3;
    const rsrc_t rws = mk_rsrc(ws, WS_END);
    const srd_t rx = mk_srd(ws, WS_END), rg = mk_srd(wg_, WFF_BYTES), ru = mk_srd(wu_, WFF_BYTES), rd = mk_srd(wd_, WFF_BYTES);
    const srd_t rs = mk_srd((n & 1) ? wu_ : wg_, WFF_BYTES);
    const unsigned smb = (unsigned)(size_t)sm;

    unsigned so[3];
    const int i0 = (1 - m) * 8 + 2 * n;
#pragma unroll
    for (int k = 0; k < 3; ++k) {
        const int ins = (k < 2) ? i0 + k : 16 + (n >> 1);
        const int qq = ins * 64 + lane;
        const int p = qq / 36, c = qq - p * 36;
        so[k] = (unsigned)(p * 9216 + (c ^ (((p >> 3) & 1) << 1)) * 16 + fs * 576);
    }
    const unsigned lw = smb + i0 * 1024, lx = smb + (n & 1) * 18432 + (16 + (n >> 1)) * 1024, lt = smb + 36864 + w * 1024;
    const unsigned l16 = lane * 16 + WS_X1 + (T * 8 + w) * 1024;
    const unsigned sd = (unsigned)((lane >> 3) * 1152 + ((lane & 7) ^ (((lane >> 4) & 3) << 1)) * 16);
    const int d0 = m ? 4 * n : 16 + 5 * n;
    const unsigned dsb = (unsigned)((fs * 288 + d0 * 8) * 1152), dlb = smb + d0 * 1024;

#define C_W(k, ks, ts, buf) do { \
        if ((k) == 0) dma(rg, so[0], ks, 0, lw + (buf), 0); \
        if ((k) == 1) dma(ru, so[0], ks, 0, lw + (buf), 18432); \
        if ((k) == 2) dma(rg, so[1], ks, 0, lw + (buf), 1024); \
        if ((k) == 3) dma(ru, so[1], ks, 0, lw + (buf), 18432 + 1024); \
        if ((k) == 4) { if (m == 0) dma(rs, so[2], ks, 0, lx + (buf), 0); } \
        if ((k) == 5) dma<SC1>(rx, l16, ts, 0, lt + (buf), 0); \
        } while (0)
#define C_D(k, b, unused, buf) do { \
        if ((k) < 4) dma(rd, sd, dsb, (k) * 9216 + (b) * 128, dlb, (buf) + (k) * 1024); \
        if ((k) == 4) { if (m == 0) dma(rd, sd, dsb, (k) * 9216 + (b) * 128, dlb, (buf) + (k) * 1024); } \
        } while (0)
#define C_DALL(b, buf) do { C_D(0, b, 0, buf); C_D(1, b, 0, buf); C_D(2, b, 0, buf); C_D(3, b, 0, buf); C_D(4, b, 0, buf); } while (0)
#define C_NONE(k, a, b, buf) do { } while (0)

    if (w != 7) {
#pragma unroll
        for (int k = 0; k < 5; ++k) { C_W(k, 0u, 0u, 0); }
#pragma unroll
        for (int k = 0; k < 5; ++k) { C_W(k, 294912u, 0u, C_BUF); }
    }
    if (WARMF && l == 0 && w >= 4 && w < 7) ffn_warm(sm, wg_, wu_, wd_, lane, bid, w + 2);
    const int rowoff = (8 * g + q) * 576 + pp * 8;
    int be = rowoff + 128 * n + 32 * (g & 1);
    int bo = rowoff + 128 * n - 32 * (g & 1);
    int bm = (8 * g + q) * 576 + (((32 + n) ^ (2 * (g & 1))) * 16) + 8 * (pp & 1) + (pp >> 1) * 18432;
    int bt = 36864 + m * 4096 + lane * 16;
    asm volatile("" : "+v"(be), "+v"(bo), "+v"(bm), "+v"(bt));
    f32x4 acc[9][4];
#pragma unroll
    for (int j = 0; j < 9; ++j)
#pragma unroll
        for (int i = 0; i < 4; ++i) {
            f32x2 z0, z1;
            asm volatile("v_mov_b64 %0, 0\n\tv_mov_b64 %1, 0" : "=v"(z0), "=v"(z1));
            acc[j][i] = __builtin_shufflevector(z0, z1, 0, 1, 2, 3);
        }
    if (w == 7) poll<true>(rws, (FL_B + l) * 2048 + (T * 16 + (lane & 15)) * 8, tag);
    else BAR();
    C_W(5, 0u, 0u, 0);
    if (w == 7) {
#pragma unroll
        for (int k = 0; k < 4; ++k) { C_W(k, 0u, 0u, 0); }
#pragma unroll
        for (int k = 0; k < 4; ++k) { C_W(k, 294912u, 0u, C_BUF); }
    }
    C_W(5, 0u, 131072u, C_BUF);

#define LORD 0
#define C_RT() do { _Pragma("unroll") for (int i = 0; i < 4; ++i) tk[i] = lds_frag(b + bt + i * 1024); CFENCE(); } while (0)
#define C_RW(j) do { lptr p = b + (((j) & 1) ? bo : be) + 32 * (j); wf[j] = lds_tr2(p, p + 2304); wf[4 + (j)] = lds_tr2(p + 18432, p + 18432 + 2304); CFENCE(); } while (0)
#define C_RM() do { wf[8] = lds_tr2(b + bm, b + bm + 2304); CFENCE(); } while (0)
#define C_LOAD1(cur, ST, a0, a1, a2) do { \
        lptr b = sm + (cur); \
        if (LORD == 0) { ST(0, a0, a1, a2); C_RT(); ST(1, a0, a1, a2); C_RW(0); ST(2, a0, a1, a2); C_RW(1); ST(3, a0, a1, a2); C_RW(2); ST(4, a0, a1, a2); C_RW(3); ST(5, a0, a1, a2); C_RM(); } \
        if (LORD == 1) { ST(0, a0, a1, a2); C_RT(); ST(1, a0, a1, a2); C_RW(0); C_RW(1); ST(2, a0, a1, a2); C_RW(2); C_RW(3); ST(3, a0, a1, a2); C_RM(); ST(5, a0, a1, a2); ST(4, a0, a1, a2); } \
        if (LORD == 2) { ST(0, a0, a1, a2); ST(1, a0, a1, a2); C_RT(); C_RW(0); ST(2, a0, a1, a2); C_RW(1); C_RW(2); ST(3, a0, a1, a2); C_RW(3); C_RM(); ST(5, a0, a1, a2); ST(4, a0, a1, a2); } \
        if (LORD == 3) { ST(0, a0, a1, a2); C_RT(); ST(1, a0, a1, a2); C_RW(0); ST(2, a0, a1, a2); C_RW(1); C_RW(2); ST(3, a0, a1, a2); C_RW(3); ST(5, a0, a1, a2); C_RM(); ST(4, a0, a1, a2); } \
    } while (0)
#define C_STEP1(cur, ST, a0, a1, a2, NX, NY, EXTRA) do { \
        bf16x8 tk[4], wf[9]; \
        C_LOAD1(cur, ST, a0, a1, a2); \
        EXTRA; \
        if (m == 1) WAIT_V(NY); \
        WAIT_L0(); SCHED(); BAR(); \
        __builtin_amdgcn_s_setprio(1); \
        _Pragma("unroll") for (int j = 0; j < 9; ++j) \
            _Pragma("unroll") for (int i = 0; i < 4; ++i) mfma_acc(acc[j][i], wf[j], tk[i]); \
        __builtin_amdgcn_s_setprio(0); \
        SCHED(); \
        if (m == 0) WAIT_V(NX); \
        BAR(); \
    } while (0)

    WAIT_V(1);
    BAR();
    if (m == 1) BAR();
    unsigned l128 = lane * 128;
    const unsigned wdo = (unsigned)(fs * 331776 + T * 20736), wqo = WS_WQF + (bid >> 5) * 258048 + ((bid >> 3) & 3) * 64512;
    int cur = 0;
#pragma unroll 1
    for (unsigned s = 2; s < 18; ++s) {
        const int nxt2 = (cur == 0) ? 2 * C_BUF : cur - C_BUF;
        C_STEP1(cur, C_W, s * 294912u, s * 131072u, nxt2, 6, 5,
                if (w == 0) {
                    if (WARM1 && s < 5) warm(rd, l128, wdo + (s < 4 ? (s - 2) * 8192 : 12544), smb + 135168);
                    if (WARM2 && s >= 10 && l == 0) warm(rx, l128, wqo + (s < 17 ? (s - 10) * 8192 : 56320), smb + 135168);
                });
        cur = (cur == 2 * C_BUF) ? 0 : cur + C_BUF;
    }
    C_STEP1(C_BUF, C_D, 0, 0, 0, 5, 4, );
    lptr hb = sm + (m ? C_S3 : C_BUF);
    const int lane2 = opaque_lane();
    const int t2 = lane2 & 15, g2 = lane2 >> 4;
    {
        bf16x8 tk[4], wf[9];
        C_LOAD1(2 * C_BUF, C_NONE, 0, 0, 0);
        lptr hm = hb + ((g2 >> 1) * 9 + 8) * 1024 + (2 * (n & 1) + (g2 & 1)) * 256 + t2 * 16 + (n >> 1) * 8;
        lptr hw = hb + 2 * n * 1024 + lane2 * 16;
        asm volatile("" : "+v"(hm), "+v"(hw));
        if (m == 1) WAIT_V(0);
        WAIT_L0(); SCHED(); BAR();
        __builtin_amdgcn_s_setprio(1);
#pragma unroll
        for (int i = 0; i < 4; ++i) { acc[8][i] = mfma16(wf[8], tk[i], acc[8][i]); SCHED(); if (m == 1) C_D(i, 1, 0, C_S2); SCHED(); }
        u32x4 o;
        float p0 = 0.f, p1 = 0.f;
#pragma unroll
        for (int z = 0; z < 37; ++z) {
            const int k = z >> 2, e = z & 3;
            if (k < 8) { const int j = 2 * (k >> 2) + (e >> 1) + 4 * (e & 1), i = k & 3; acc[j][i] = mfma16(wf[j], tk[i], acc[j][i]); }
            SCHED();
            if (z > 0) {
                const int kp = (z - 1) >> 2, ep = (z - 1) & 3;
                o[ep] = pack2(p0, p1);
                if (ep == 3) {
                    if (kp == 0) { *(LAS u32x2*)(hm) = (u32x2){o[0], o[1]}; *(LAS u32x2*)(hm + 18432) = (u32x2){o[2], o[3]}; }
                    else *(LAS u32x4*)(hw + (((kp - 1) & 3) * 9 + ((kp - 1) >> 2)) * 1024) = o;
                }
            }
            SCHED();
            if (k == 0) {
                const int i = e & 2, r = 2 * (e & 1);
                auto x = __builtin_amdgcn_permlane32_swap(__float_as_uint(acc[8][i][r]), __float_as_uint(acc[8][i + 1][r]), false, false);
                auto y = __builtin_amdgcn_permlane32_swap(__float_as_uint(acc[8][i][r + 1]), __float_as_uint(acc[8][i + 1][r + 1]), false, false);
                p0 = reglu(__uint_as_float(x[0]), __uint_as_float(x[1])); p1 = reglu(__uint_as_float(y[0]), __uint_as_float(y[1]));
            } else if (k < 9) {
                const int j = 2 * ((k - 1) >> 2) + (e >> 1), i = (k - 1) & 3, r = 2 * (e & 1);
                p0 = reglu(acc[j][i][r], acc[4 + j][i][r]); p1 = reglu(acc[j][i][r + 1], acc[4 + j][i][r + 1]);
            }
            SCHED();
        }
        __builtin_amdgcn_s_setprio(0);
        if (m == 0) WAIT_V(0);
        WAIT_L0(); SCHED(); BAR();
    }
    bf16x8 hf[4][9];
    const int bd = (lane2 >> 2) * 128 + ((n ^ ((lane2 >> 3) & 3)) * 32) + (lane2 & 3) * 8;
    const unsigned po = (unsigned)((t2 >> 2) * 9216 + g2 * 64 + (t2 & 3) * 16);
    const unsigned pb = WS_PART + fs * ACT_BYTES + (T * 2 + m) * 73728 + n * 256;
    const unsigned evo = (unsigned)g2 * ACT_BYTES + t2 * 16, esb = WS_PART + (T * 16 + fs) * 9216 + w * 256, elb = smb + w * 4096;
    const unsigned fme = (FL_C + l) * 2048 + (T * 16 + fs) * 8, fpeer = (FL_C + l) * 2048 + (T * 16 + t2) * 8;
    u64 chk = 0;
    u32x2 esv, eres[3]; unsigned emean;
    f32x4 a2[4];
#define C_STORE(b) do { \
        _Pragma("unroll") for (int ip = 0; ip < 2; ++ip) { \
            u32x4 o; \
            o[0] = pack2(a2[2 * ip][0], a2[2 * ip][1]); o[1] = pack2(a2[2 * ip][2], a2[2 * ip][3]); \
            o[2] = pack2(a2[2 * ip + 1][0], a2[2 * ip + 1][1]); o[3] = pack2(a2[2 * ip + 1][2], a2[2 * ip + 1][3]); \
            st16(rws, po, pb + ip * 36864 + (b) * 1024, o); } \
        CFENCE(); \
    } while (0)
#pragma unroll
    for (int b = 0; b < 9; ++b) {
        bf16x8 wf[9];
        lptr pw = sm + C_SLOT(b) + bd;
        if (b == 0) {
            if (m == 0) C_DALL(1, C_S2); else C_DALL(2, C_SLOT(2));
#pragma unroll
            for (int s = 0; s < 9; ++s) wf[s] = lds_tr2(pw + s * 4096, pw + s * 4096 + 2048);
#pragma unroll
            for (int s = 0; s < 9; ++s)
#pragma unroll
                for (int i = 0; i < 4; ++i) hf[i][s] = lds_frag(hb + lane2 * 16 + (i * 9 + s) * 1024);
        } else {
            if (EARLY && b == 7) { if (w == 0) set_flag(rws, fme, tag + 1, lane2); }
            if (b == 1) C_DALL(3, C_SLOT(3));
#pragma unroll
            for (int s = 0; s < 9; ++s) {
                if (b + 3 < 9 && (s & 1) == 0) C_D(s >> 1, b + 3, 0, C_SLOT(b + 3));
                wf[s] = lds_tr2(pw + s * 4096, pw + s * 4096 + 2048);
                CFENCE();
            }
            if (b == 6) {
                const int R = T * 16 + fs, tok = (R >> 2) * 32 + (R & 3) * 4 + (t2 & 3) + (g2 & 1) * 16;
                esv = ld8<SC1>(rws, (unsigned)(tok * 64 + ((t2 >> 2) + 4 * (g2 >> 1)) * 8), WS_STATS);
                emean = ld4<SC1>(rws, (unsigned)(tok * 4), WS_MEAN);
#pragma unroll
                for (int k = 0; k < 3; ++k) {
                    const int bn = k < 2 ? 16 * k + 8 * (g2 >> 1) + w : 32 + n;
                    const int d = 16 * bn + 4 * (t2 >> 2);
                    ep.xo[k] = (unsigned)(tok * 1152 + d * 2);
                    eres[k] = ld8<SC1>(rws, (unsigned)((d >> 5) * 131072 + (tok >> 4) * 1024 + ((d >> 3) & 3) * 256 + (tok & 15) * 16 + (d & 7) * 2), WS_X1);
                    asm volatile("" : "+v"(ep.xo[k]));
                }
                CFENCE();
            }
            if (b == 8) {
                float sq = __uint_as_float(esv[0]) + __uint_as_float(esv[1]);
                sq += shfl(sq, lane2 ^ 4); sq += shfl(sq, lane2 ^ 8); sq += shfl(sq, lane2 ^ 32);
                ep.rsq = __builtin_amdgcn_rcpf(sq * (1.0f / D_) + LN_EPS);
#pragma unroll
                for (int k = 0; k < 3; ++k) {
                    ep.r0[k] = (f32x4){bf_lo(eres[k][0]), bf_hi(eres[k][0]), bf_lo(eres[k][1]), bf_hi(eres[k][1])} + __uint_as_float(emean);
                    asm volatile("" : "+v"(ep.r0[k]));
                }
                asm volatile("" : "+v"(ep.rsq));
            }
            C_STORE(b - 1);
            if (EARLY && b == 8) { if (w == 7) { if (!__all(chk - (tag + 1) <= 1)) poll(rws, fpeer, tag + 1, 1); } }
        }
        if (m == 1) {
            if (b == 0) WAIT_V(4); else if (b == 1) WAIT_V(10); else if (b == 2) WAIT_V(12); else if (b >= 3 && b <= 5) WAIT_V(14); else if (b == 6) WAIT_V(8); else if (b == 7) WAIT_V(6);
        }
        WAIT_L0();
        SCHED();
        BAR();
        if (b == 0) { if (m == 0) C_DALL(2, C_SLOT(2)); }
        if (EARLY && b == 7) { if (w == 7) chk = flag_ld(rws, fpeer); }
        __builtin_amdgcn_s_setprio(1);
#pragma unroll
        for (int i = 0; i < 4; ++i) a2[i] = mfma16(wf[0], hf[i][0], (f32x4){0.f, 0.f, 0.f, 0.f});
#pragma unroll
        for (int s = 1; s < 9; ++s)
#pragma unroll
            for (int i = 0; i < 4; ++i) a2[i] = mfma16(wf[s], hf[i][s], a2[i]);
        __builtin_amdgcn_s_setprio(0);
        SCHED();
        if (m == 0) {
            if (b == 0) WAIT_V(5); else if (b == 1) WAIT_V(12); else if (b == 2) WAIT_V(14); else if (b >= 3 && b <= 5) WAIT_V(16); else if (b == 6) WAIT_V(9); else if (b == 7) WAIT_V(6);
        }
        if (b < 8 || m == 0) BAR();
    }
    C_STORE(8);
    if (EARLY && w != 7) { E_FETCH(0, C_SLOT(5)); E_FETCH(1, C_SLOT(6)); WAIT_V(8); } else { WAIT_V(0); }
    BAR();
    if (w == 7) set_flag(rws, fme, tag + 2, lane2);
#undef C_W
#undef C_D
#undef C_DALL
#undef C_NONE
#undef C_LOAD1
#undef C_STEP1
#undef C_STORE
}

DI bool stage_e(bf16x8 (&we)[18][2], const EPre& ep, lptr sm, char* ws, char* xout, const int l_, const int w_, u64 tag) {
    asm volatile("" : "+s"(tag));
    const int lane = opaque_lane();
    const int bid = opaque_s(blockIdx.x), w = opaque_s(w_), l = opaque_s(l_);
    const int fs = 2 * (bid & 7) + ((bid >> 3) & 1), T = bid >> 4;
    const int R = T * 16 + fs;
    const int t = lane & 15, g = lane >> 4;
    const rsrc_t rws = mk_rsrc(ws, WS_END), ro = mk_rsrc(xout, ACT_BYTES);
    const srd_t rx = mk_srd(ws, WS_END);
    const unsigned evo = (unsigned)g * ACT_BYTES + t * 16, esb = WS_PART + R * 9216 + w * 256, elb = (unsigned)(size_t)sm + w * 4096;
    const unsigned fpeer = (FL_C + l) * 2048 + (T * 16 + t) * 8;
    const int e4 = 36864 + (w >> 1) * (81920 - 36864 - 8192);

    u32x4 sel[2];
#pragma unroll
    for (int e = 0; e < 4; ++e) {
        sel[0][e] = (t == 2 * e) ? 0x00003f80u : ((t == 2 * e + 1) ? 0x3f800000u : 0u);
        sel[1][e] = (t == 8 + 2 * e) ? 0x00003f80u : ((t == 9 + 2 * e) ? 0x3f800000u : 0u);
    }
    asm volatile("" : "+v"(sel[0]), "+v"(sel[1]));
    if (l == 0) a_we(we, ws, lane, bid >> 5, w, w < 7, 0, E_N1);
    else {
#pragma unroll
        for (int s = 0; s < E_N1; ++s) asm volatile("" : "=v"(we[s][0]), "=v"(we[s][1]));
    }
#define E_SUM(k, s0, s1) do { \
        bf16x8 v[8]; \
        _Pragma("unroll") for (int f = 0; f < ((k) < 2 ? 8 : 4); ++f) v[f] = lds_frag(sm + (f < 4 ? (s0) : (s1)) + w * 4096 + (f & 3) * 1024 + lane * 16); \
        SCHED(); \
        f32x4 a = {0.f, 0.f, 0.f, 0.f}; \
        _Pragma("unroll") for (int f = 0; f < ((k) < 2 ? 8 : 4); ++f) a = mfma16(as_frag(sel[f >> 2]), v[f], a); \
        a = a * ep.rsq + ep.r0[k]; \
        if ((k) < 2 || g < 2) st8(ro, ep.xo[k], 0, (u32x2){pack2(a[0], a[1]), pack2(a[2], a[3])}); \
    } while (0)
    if (w == 7) poll<true>(rws, fpeer, tag + 2, 0);
    else BAR();
    if (!EARLY || w == 7) { E_FETCH(0, C_SLOT(5)); E_FETCH(1, C_SLOT(6)); }
    E_FETCH(2, C_SLOT(7));
    E_FETCH(3, C_SLOT(8));
    if (w < 4) { const unsigned elb4 = elb + e4; _Pragma("unroll") for (int f = 0; f < 4; ++f) dma<SC1>(rx, evo, esb, f * 4 * (int)ACT_BYTES + 8192, elb4, f * 1024); }
    if (w < 4) WAIT_V(12); else WAIT_V(8);
    E_SUM(0, C_SLOT(5), C_SLOT(6));
    WAIT_V(1);
    SCHED();
    E_SUM(1, C_SLOT(7), C_SLOT(8));
    if (w < 4) E_SUM(2, e4, 0);
    if (l) { return true; }
    publish(rws, FL_E * 2048 + R * 8, tag, w, lane);
    return false;
#undef E_SUM
}

__global__ __launch_bounds__(512, 2) void fused_layers(const char* __restrict__ x0, const char* __restrict__ wqkv, const char* __restrict__ wo,
                                                       const char* __restrict__ wg, const char* __restrict__ wu, const char* __restrict__ wd,
                                                       char* __restrict__ out, char* __restrict__ ws) {
    extern __shared__ __attribute__((aligned(16))) char smem_[];
    lptr sm = (lptr)smem_;
    const int w = __builtin_amdgcn_readfirstlane(threadIdx.x >> 6);
    u64 tag = *(const __attribute__((address_space(4))) u64*)(ws + WS_GEN) + 0x9E3779B97F4A7C15ull;
    bf16x8 we[18][2];
    stage_0(we, sm, ws, x0, wqkv, wo, wg, wu, wd, w, tag);
#pragma unroll 1
    for (int l = 0;; ++l) {
        f32x4 acc[2][5];
        APre pre;
        a_gemm(acc, pre, we, sm, ws, wg, wu, wd, l, w, tag);
        a_post(acc, pre, sm, ws, l, w, tag);
        stage_b(sm, ws, l == 0 ? x0 : ws + WS_X2, l, w, tag);
        EPre ep;
        stage_c(ep, sm, ws, wg, wu, wd, l, w, tag);
        if (stage_e(we, ep, sm, ws, l == 0 ? ws + WS_X2 : out, l, w, tag)) break;
    }
    if (blockIdx.x == 0 && w == 0) {
        const int lane = opaque_lane();
        const rsrc_t rws = mk_rsrc(ws, WS_END);
#pragma unroll
        for (int k = 0; k < 4; ++k) poll(rws, FL_A * 2048 + (k * 64 + lane) * 8, tag);
        if (lane == 0) st8(rws, 0, WS_GEN, __builtin_bit_cast(u32x2, tag));
        asm volatile(".Lfl_code_end:");
    }
}

extern "C" void kernel_launch(void* const* d_in, const int* in_sizes, int n_in, void* d_out, int out_size, void* d_ws, size_t ws_size,
                              hipStream_t stream) {
    (void)in_sizes; (void)n_in; (void)out_size; (void)ws_size;
    (void)hipFuncSetAttribute((const void*)fused_layers, hipFuncAttributeMaxDynamicSharedMemorySize, 163840);
    fused_layers<<<256, 512, 163840, stream>>>((const char*)d_in[0], (const char*)d_in[1], (const char*)d_in[2], (const char*)d_in[4],
                                               (const char*)d_in[5], (const char*)d_in[6], (char*)d_out, (char*)d_ws);
}
```

```cpp
#include <hip/hip_runtime.h>
#include <hip/hip_bf16.h>

#define LAS __attribute__((address_space(3)))
typedef __bf16 bf16x8 __attribute__((ext_vector_type(8)));
typedef __bf16 bf16x4 __attribute__((ext_vector_type(4)));
typedef __bf16 bf16x2 __attribute__((ext_vector_type(2)));
typedef float f32x4 __attribute__((ext_vector_type(4)));
typedef float f32x2 __attribute__((ext_vector_type(2)));
typedef unsigned u32x4 __attribute__((ext_vector_type(4)));
typedef unsigned u32x2 __attribute__((ext_vector_type(2)));
typedef unsigned long long u64;
typedef LAS char* lptr;
typedef __amdgpu_buffer_rsrc_t rsrc_t;

#define DI __device__ __forceinline__
#define WAIT_V(n) asm volatile("s_waitcnt vmcnt(" #n ")" ::: "memory")
#define WAIT_V0() do { asm volatile("s_waitcnt vmcnt(0)" ::: "memory"); __builtin_amdgcn_s_waitcnt(0x0F70); asm volatile("" ::: "memory"); } while (0)
#define WAIT_L0() do { asm volatile("" ::: "memory"); __builtin_amdgcn_s_waitcnt(0xC07F); asm volatile("" ::: "memory"); } while (0)
#define CFENCE() asm volatile("" ::: "memory")
#define BAR() do { CFENCE(); __builtin_amdgcn_s_barrier(); CFENCE(); } while (0)
#define SCHED() __builtin_amdgcn_sched_barrier(0)
#define SC1 16

constexpr int S_ = 2048, D_ = 576, F_ = 4608;
constexpr float LN_EPS = 1e-5f;
constexpr unsigned ACT_BYTES = (unsigned)S_ * D_ * 2;
constexpr unsigned WFF_BYTES = (unsigned)D_ * F_ * 2;
constexpr unsigned WQKV_BYTES = (unsigned)D_ * 3 * D_ * 2, WO_BYTES = (unsigned)D_ * D_ * 2;

constexpr unsigned WS_GEN = 0;
constexpr unsigned WS_FLAGS = 262144, FSC = 8;
constexpr unsigned FL_A = 0, FL_B = 2, FL_C = 4, FL_E = 6, FL_S = 7;
constexpr unsigned FL_Q = 8 * 2048;
constexpr unsigned WS_MEAN = 65536;
constexpr unsigned WS_STATS = 131072;
constexpr unsigned WS_WOF = 3u << 18;
constexpr unsigned WS_WQF = WS_WOF + 8 * 92160;
constexpr unsigned WS_O = WS_WQF + 8 * 258048;
constexpr unsigned WS_X1 = WS_O + ACT_BYTES;
constexpr unsigned WS_X2 = WS_X1 + ACT_BYTES;
constexpr unsigned WS_PART = WS_X2 + ACT_BYTES;
constexpr unsigned WS_END = WS_PART + 16 * ACT_BYTES;

DI rsrc_t mk_rsrc(const void* p, unsigned bytes) { return __builtin_amdgcn_make_buffer_rsrc((void*)p, 0, bytes, 0x00020000); }
typedef unsigned srd_t __attribute__((ext_vector_type(4)));
DI srd_t mk_srd(const void* p, unsigned bytes) {
    const u64 a = (u64)p;
    return (srd_t){(unsigned)a, (unsigned)(a >> 32) & 0xffffu, bytes, 0x00020000u};
}
template <int AUX = 0> DI void dma(srd_t r, unsigned voff, unsigned sb, int si, unsigned lb, int li) {
    unsigned tmp;
    if (AUX) asm volatile("s_add_i32 m0, %3, %5\n\ts_add_i32 %0, %4, %6\n\tbuffer_load_dwordx4 %1, %2, %0 offen sc1 lds"
                          : "=&s"(tmp) : "v"(voff), "s"(r), "s"(lb), "s"(sb), "i"(li), "i"(si) : "memory", "scc");
    else     asm volatile("s_add_i32 m0, %3, %5\n\ts_add_i32 %0, %4, %6\n\tbuffer_load_dwordx4 %1, %2, %0 offen lds"
                          : "=&s"(tmp) : "v"(voff), "s"(r), "s"(lb), "s"(sb), "i"(li), "i"(si) : "memory", "scc");
}
DI void dma_if0(int c, srd_t r, unsigned voff, unsigned sb, int si, unsigned lb, int li) {
    unsigned tmp;
    asm volatile("s_cmp_lg_u32 %7, 0\n\ts_cbranch_scc1 .Ld%=\n\ts_add_i32 m0, %3, %5\n\ts_add_i32 %0, %4, %6\n\tbuffer_load_dwordx4 %1, %2, %0 offen lds\n.Ld%=:"
                 : "=&s"(tmp) : "v"(voff), "s"(r), "s"(lb), "s"(sb), "i"(li), "i"(si), "s"(c) : "memory", "scc");
}
DI void dma_masked(srd_t r, unsigned voff, unsigned sb, int si, unsigned lb, int li, u64 mask) {
    unsigned tmp;
    asm volatile("s_add_i32 m0, %3, %5\n\ts_add_i32 %0, %4, %6\n\ts_mov_b64 exec, %7\n\tbuffer_load_dwordx4 %1, %2, %0 offen sc1 lds\n\ts_mov_b64 exec, -1"
                 : "=&s"(tmp) : "v"(voff), "s"(r), "s"(lb), "s"(sb), "i"(li), "i"(si), "s"(mask) : "memory", "scc");
}
DI void warm(srd_t r, unsigned voff, unsigned soff, unsigned lb) {
    asm volatile("s_mov_b32 m0, %0\n\ts_nop 0\n\tbuffer_load_dword %1, %2, %3 offen sc1 lds" :: "s"(lb), "v"(voff), "s"(r), "s"(soff) : "memory");
}
template <int AUX> DI u32x4 ld16(rsrc_t r, unsigned voff, unsigned soff) { return __builtin_amdgcn_raw_buffer_load_b128(r, voff, soff, AUX); }
template <int AUX> DI u32x2 ld8(rsrc_t r, unsigned voff, unsigned soff) { return __builtin_amdgcn_raw_buffer_load_b64(r, voff, soff, AUX); }
DI void st16(rsrc_t r, unsigned voff, unsigned soff, u32x4 v) {
    __builtin_amdgcn_raw_buffer_store_b128(v, r, voff, soff, SC1);
    asm volatile("s_nop 1" :: "v"(v) : "memory");
}
DI void st4(rsrc_t r, unsigned voff, unsigned soff, unsigned v) { __builtin_amdgcn_raw_buffer_store_b32(v, r, voff, soff, SC1); }
template <int AUX> DI unsigned ld4(rsrc_t r, unsigned voff, unsigned soff) { return __builtin_amdgcn_raw_buffer_load_b32(r, voff, soff, AUX); }
DI void st8(rsrc_t r, unsigned voff, unsigned soff, u32x2 v) { __builtin_amdgcn_raw_buffer_store_b64(v, r, voff, soff, SC1); }
DI bf16x8 lds_frag(lptr p) { return *(const LAS bf16x8*)p; }
DI bf16x8 lds_tr2(lptr p0, lptr p1) {
    bf16x4 lo = __builtin_amdgcn_ds_read_tr16_b64_v4bf16((LAS bf16x4*)p0);
    bf16x4 hi = __builtin_amdgcn_ds_read_tr16_b64_v4bf16((LAS bf16x4*)p1);
    return __builtin_shufflevector(lo, hi, 0, 1, 2, 3, 4, 5, 6, 7);
}
DI f32x4 mfma16(bf16x8 a, bf16x8 b, f32x4 c) { return __builtin_amdgcn_mfma_f32_16x16x32_bf16(a, b, c, 0, 0, 0); }
DI void mfma_acc(f32x4& acc, bf16x8 a, bf16x8 b) { asm volatile("v_mfma_f32_16x16x32_bf16 %0, %1, %2, %0" : "+v"(acc) : "v"(a), "v"(b)); }
DI void mfma_zero(f32x4& acc, bf16x8 a, bf16x8 b) { asm volatile("v_mfma_f32_16x16x32_bf16 %0, %1, %2, 0" : "=&v"(acc) : "v"(a), "v"(b)); }
DI float shfl(float v, int src) { return __int_as_float(__builtin_amdgcn_ds_bpermute(src << 2, __float_as_int(v))); }
DI bf16x8 as_frag(u32x4 v) { return __builtin_bit_cast(bf16x8, v); }
DI unsigned pack2(float a, float b) {
    f32x2 f = {a, b};
    bf16x2 h = __builtin_convertvector(f, bf16x2);
    return __builtin_bit_cast(unsigned, h);
}
DI float bf_lo(unsigned u) { return __uint_as_float(u << 16); }
DI float bf_hi(unsigned u) { return __uint_as_float(u & 0xffff0000u); }
DI float dot2(unsigned a, unsigned b, float c) {
    return __builtin_amdgcn_fdot2_f32_bf16(__builtin_bit_cast(bf16x2, a), __builtin_bit_cast(bf16x2, b), c, false);
}
DI float fmax_raw(float a, float b) { float r; asm("v_max_f32 %0, %1, %2" : "=v"(r) : "v"(a), "v"(b)); return r; }
DI float relu(float x) { const int i = __float_as_int(x); return __int_as_float(i > 0 ? i : 0); }
DI float red_sum_g(float v) {
    auto a = __builtin_amdgcn_permlane16_swap(__float_as_uint(v), __float_as_uint(v), false, false);
    v = __uint_as_float(a[0]) + __uint_as_float(a[1]);
    auto b = __builtin_amdgcn_permlane32_swap(__float_as_uint(v), __float_as_uint(v), false, false);
    return __uint_as_float(b[0]) + __uint_as_float(b[1]);
}
DI float red_max_g(float v) {
    auto a = __builtin_amdgcn_permlane16_swap(__float_as_uint(v), __float_as_uint(v), false, false);
    v = fmax_raw(__uint_as_float(a[0]), __uint_as_float(a[1]));
    auto b = __builtin_amdgcn_permlane32_swap(__float_as_uint(v), __float_as_uint(v), false, false);
    return fmax_raw(__uint_as_float(b[0]), __uint_as_float(b[1]));
}
DI int opaque_lane() { int l; asm volatile("v_mbcnt_lo_u32_b32 %0, -1, 0\n\tv_mbcnt_hi_u32_b32 %0, -1, %0" : "=v"(l)); return l; }
DI int opaque_s(int v) { asm volatile("" : "+s"(v)); return v; }

DI float bcast_row0(float v) { return __int_as_float(__builtin_amdgcn_update_dpp(0, __float_as_int(v), 0x150, 0xf, 0xf, false)); }

DI void set_flag(rsrc_t rws, unsigned off, u64 val, int lane) {
    if (lane == 0) st8(rws, 0, WS_FLAGS + off * FSC, __builtin_bit_cast(u32x2, val));
    CFENCE();
}
DI void publish(rsrc_t rws, unsigned off, u64 val, int w, int lane) {
    WAIT_V0();
    BAR();
    if (w == 0) set_flag(rws, off, val, lane);
}
DI u64 flag_ld(rsrc_t rws, unsigned off) { CFENCE(); const u64 v = __builtin_bit_cast(u64, ld8<SC1>(rws, off * FSC, WS_FLAGS)); CFENCE(); return v; }
template <bool WITH_BAR = false> DI void poll(rsrc_t rws, unsigned off, u64 tag) {
    u64 v0, v1, v2 = 0;
    v0 = flag_ld(rws, off); __builtin_amdgcn_s_sleep(2);
    v1 = flag_ld(rws, off); __builtin_amdgcn_s_sleep(2);
#pragma unroll 1
    for (unsigned it = 0; it < (1u << 19); ++it) {
        v2 = flag_ld(rws, off); if (__all(v0 == tag)) break;
        v0 = flag_ld(rws, off); if (__all(v1 == tag)) break;
        v1 = flag_ld(rws, off); if (__all(v2 == tag)) break;
    }
    if (WITH_BAR) BAR();
    asm volatile("" :: "v"(v0), "v"(v1), "v"(v2) : "memory");
}

constexpr int A_SCR = 92160, A_SCR_RS = 1168;
constexpr int A_SCRAP = 160768;
constexpr int A_QKV = 51200, A_RS = 464;
constexpr int A_ST = 162816;
constexpr int A_MEAN = 163456;
constexpr int B_W1 = 92160, B_W2 = 0, B_N1 = 65;
constexpr int B_RED = 135168;
constexpr unsigned ONES2 = 0x3f803f80u;
constexpr int E_N1 = 16;
constexpr int SKEW = 10;

DI int a_tt(const int bid) { return (bid >> 7) * 16 + (bid & 3) * 4 + ((bid >> 3) & 3); }
DI int a_hg(const int bid) { return (bid & 4) + ((bid >> 5) & 3); }
DI int a_rot(const int tt, const int hg) { const int v = tt + hg; return v - 3 * ((v * 43) >> 7); }
struct ACtx { int lane, w, tt, hg, rot; lptr ta0, ta1; unsigned xo, xs[2], xl[2]; u64 xm; };
DI ACtx a_ctx(lptr sm, const int w_) {
    ACtx c;
    c.lane = opaque_lane();
    const int bid = opaque_s(blockIdx.x);
    c.w = opaque_s(w_);
    c.tt = a_tt(bid); c.hg = a_hg(bid);
    c.rot = a_rot(c.tt, c.hg);
    const int t = c.lane & 15, g = c.lane >> 4;
    c.ta0 = sm + t * 128 + ((g ^ (t & 7)) * 16); c.ta1 = sm + t * 128 + (((4 + g) ^ (t & 7)) * 16);
    c.xo = (unsigned)((c.lane >> 3) * 1152 + ((c.lane & 7) ^ (c.lane >> 3)) * 16);
    const int rb = (c.w + 5) >> 3, kq = c.w - 3 * ((c.w * 11) >> 5);
    c.xs[0] = (unsigned)((c.tt * 64 - (c.tt ? 16 : 0) + rb * 8) * 1152 + kq * 128);
    c.xl[0] = (unsigned)(size_t)sm + (kq * 10 + rb) * 1024;
    c.xs[1] = (unsigned)((c.tt * 64 + c.w * 8) * 1152);
    c.xl[1] = (unsigned)(size_t)sm + (c.w + 2) * 1024;
    c.xm = c.w < 3 ? ~0xffull : ~0ull;
    return c;
}
DI void a_xdma(const ACtx& c, const srd_t rx, const int third) {
    const int src = third + c.rot;
    const unsigned vo = c.xo + (src >= 3 ? src - 3 : src) * 384;
    dma_masked(rx, vo, c.xs[0], 0, c.xl[0], third * 30720, c.xm);
#pragma unroll
    for (int i = 0; i < 3; ++i) dma<SC1>(rx, vo, c.xs[1], i * 128, c.xl[1], third * 30720 + i * 10240);
}
DI bf16x8 a_ones_frag() {
    u32x4 o;
#pragma unroll
    for (int e = 0; e < 4; ++e) asm volatile("v_mov_b32 %0, 0x3f803f80" : "=v"(o[e]));
    return as_frag(o);
}
DI void a_ones(lptr sm, const int lane) {
    const bf16x8 o = a_ones_frag();
    if (lane < 8) {
#pragma unroll
        for (int kb = 0; kb < 9; ++kb) *(LAS bf16x8*)(sm + kb * 10240 + lane * 16) = o;
    }
}
DI void a_tok(bf16x8 (&tk)[5], const ACtx& c, const int s) {
    lptr ta = ((s & 1) ? c.ta1 : c.ta0) + (s >> 1) * 10240;
#pragma unroll
    for (int i = 0; i < 5; ++i) tk[i] = lds_frag(ta + i * 2048);
}
template <bool STATS> DI void a_mm(f32x4 (&acc)[2][5], const bf16x8 (&tk)[5], const bf16x8 f0, const bf16x8 f1, const int s, const int i0, const int i1,
                                   bf16x8 (&tn)[5], const ACtx& c) {
    const f32x4 z = {0.f, 0.f, 0.f, 0.f};
    lptr ta = (((s + 1) & 1) ? c.ta1 : c.ta0) + ((s + 1) >> 1) * 10240;
#pragma unroll
    for (int i = i0; i < i1; ++i) {
        SCHED();
        if (s + 1 < 18) tn[i] = lds_frag(ta + i * 2048);
        SCHED();
        const bf16x8 g1 = STATS ? tk[i] : f1;
        if (s == 0) { acc[0][i] = mfma16(f0, tk[i], z); acc[1][i] = mfma16(g1, tk[i], z); }
        else if (s == 17) { acc[0][i] = mfma16(f0, tk[i], acc[0][i]); acc[1][i] = mfma16(g1, tk[i], acc[1][i]); }
        else if (i & 1) { mfma_acc(acc[1][i], g1, tk[i]); mfma_acc(acc[0][i], f0, tk[i]); }
        else { mfma_acc(acc[0][i], f0, tk[i]); mfma_acc(acc[1][i], g1, tk[i]); }
    }
}
DI void a_we(bf16x8 (&we)[18][2], char* ws, const int lane, const int bid, const int w, const bool on, const int s0, const int s1) {
    const int hg = a_hg(bid), rot = a_rot(a_tt(bid), hg);
    const rsrc_t r = mk_rsrc(ws + WS_WQF + (hg * 18 * 14 + 2 * w) * 1024, 258048u);
    if (on) {
#pragma unroll
        for (int s = s0; s < s1; ++s) {
            const int src = s / 6 + rot;
            const unsigned vo = lane * 16 + (src >= 3 ? src - 3 : src) * 86016;
#pragma unroll
            for (int j = 0; j < 2; ++j) we[s][j] = as_frag(ld16<SC1>(r, vo, ((s % 6) * 14 + j) * 1024));
        }
    } else {
#pragma unroll
        for (int s = s0; s < s1; ++s) asm volatile("" : "=v"(we[s][0]), "=v"(we[s][1]));
    }
    CFENCE();
}
DI void a_stats_out(const f32x4 (&acc)[2][5], lptr sm, const int lane) {
    const int t = lane & 15, g = lane >> 4;
    if (g == (t >> 2)) {
#pragma unroll
        for (int i = 0; i < 5; ++i) {
            const float sq = (t & 2) ? ((t & 1) ? acc[1][i][3] : acc[1][i][2]) : ((t & 1) ? acc[1][i][1] : acc[1][i][0]);
            const float mean = acc[0][i][0] * (1.0f / D_);
            const float rstd = __builtin_amdgcn_rsqf(fmaxf(sq * (1.0f / D_) - mean * mean, 0.0f) + LN_EPS);
            *(LAS f32x2*)(sm + A_ST + (16 * i + t) * 8) = (f32x2){mean * rstd, rstd};
            *(LAS float*)(sm + A_MEAN + (16 * i + t) * 4) = mean;
        }
    }
}
struct APre { f32x4 bias[2]; unsigned ow, ost, rb, vb, oo[3]; u32x2 res[3]; };
DI void a_res(APre& p, unsigned (&ra)[3], lptr sm, const ACtx& c, const int pass) {
    const int t = c.lane & 15, g = c.lane >> 4, lr = 16 + (c.w & 3) * 16 + t;
#pragma unroll
    for (int j = 0; j < 3; ++j) {
        if (pass == 0) {
            int d = c.hg * 72 + (j < 2 ? 32 * (c.w >> 2) + 16 * j + 4 * g : 64 + 4 * (g & 1)) - 192 * c.rot;
            d = d < 0 ? d + 576 : d;
            ra[j] = (unsigned)((d >> 6) * 10240 + lr * 128 + ((((d >> 3) ^ t) & 7) * 16) + (d & 4) * 2);
            p.res[j] = *(const LAS u32x2*)(sm + ra[j]);
        } else {
            const u32x2 v = *(const LAS u32x2*)(sm + ra[j]);
            p.res[j][0] = ra[j] < 30720 ? p.res[j][0] : v[0]; p.res[j][1] = ra[j] < 30720 ? p.res[j][1] : v[1];
        }
    }
    CFENCE();
}
DI void a_pre(APre& p, const ACtx& c) {
    const int lane = c.lane, t = lane & 15, g = lane >> 4, i = c.w >> 1, hp = c.w & 1;
    const int pos = c.tt * 64 + 16 * i + t;
    const unsigned V = 0x5555u & ((2u << (pos < 14 ? pos : 14)) - 1u);
    const int sh = 13 + t - 4 * g;
    const unsigned U[2] = {~(V >> sh), ~((V << 16) >> sh)};
#pragma unroll
    for (int a = 0; a < 2; ++a)
#pragma unroll
        for (int r = 0; r < 4; ++r) p.bias[a][r] = __uint_as_float((unsigned)__builtin_amdgcn_sbfe((int)U[a], 3 - r, 1) & 0xf149f2cau);
    p.ow = (unsigned)(A_QKV + t * A_RS + c.w * 64 + g * 8);
    p.ost = (unsigned)(A_ST + t * 8);
    p.rb = (unsigned)(A_QKV + (16 * i + t) * A_RS + hp * 72 + g * 16);
    p.vb = (unsigned)(A_QKV + (16 * i + (lane >> 2)) * A_RS + 288 + hp * 72 + (lane & 3) * 8);
#pragma unroll
    for (int k = 0; k < 3; ++k) {
        const int d = c.hg * 72 + hp * 36 + 16 * k + 4 * g;
        p.oo[k] = (unsigned)((d >> 5) * 131072 + ((d >> 3) & 3) * 256 + t * 16 + (d & 7) * 2);
    }
    asm volatile("" : "+v"(p.bias[0]), "+v"(p.bias[1]), "+v"(p.ow), "+v"(p.ost), "+v"(p.rb), "+v"(p.vb), "+v"(p.oo[0]), "+v"(p.oo[1]), "+v"(p.oo[2]));
}

DI void ffn_warm(lptr sm, const char* wg_, const char* wu_, const char* wd_, const int lane, const int bid, const int k) {
    const int lq = lane < 54 ? lane : 53;
    const unsigned scrap = (unsigned)(size_t)sm + A_SCRAP;
    if (k < 6) warm(mk_srd((k & 1) ? wu_ : wg_, WFF_BYTES), (unsigned)((lq / 9) * 9216 + (lq % 9) * 128), (unsigned)(((bid >> 3) * 6 + (k >> 1) * 192) * 9216 + (bid & 7) * 1152), scrap);
    else warm(mk_srd(wd_, WFF_BYTES), lq * 128, (unsigned)((bid & 7) * 663552 + (bid >> 3) * 20736 + (k - 6) * 6912), scrap);
}

DI void stage_0(bf16x8 (&we)[18][2], lptr sm, char* ws, const char* xin, const char* wqkv_, const char* wo_, const int w_, u64& tag) {
    const int lane = opaque_lane(), w = w_;
    const int bid = blockIdx.x;
    const int tt = a_tt(bid), hg = a_hg(bid);
    const rsrc_t rws = mk_rsrc(ws, WS_END);
    const unsigned lw = (unsigned)((lane >> 3) * A_SCR_RS + (lane & 7) * 16), lr = (unsigned)(((lane >> 2) & 3) * A_SCR_RS);
    if (w < 7 && tt < 18) {
        const srd_t rw = mk_srd(wqkv_, WQKV_BYTES);
        const unsigned dst = (unsigned)(size_t)sm + A_SCR + w * 2048;
#pragma unroll
        for (int i = 0; i < 2; ++i) {
            const int p = i * 16 + (lane >> 2);
            const int cc = 4 * w + ((lane & 3) ^ (((p >> 3) & 1) << 1));
            const int mat = (cc >= 18) ? 2 : (cc >= 9 ? 1 : 0);
            dma(rw, cc > 26 ? 0x7ffffff0u : (unsigned)(p * 3456 + mat * (1152 - 144) + cc * 16), (unsigned)(tt * 32 * 3456 + hg * 144), 0, dst, i * 1024);
        }
    }
    const ACtx c = a_ctx(sm, w);
    const srd_t rx = mk_srd(xin, ACT_BYTES);
    a_xdma(c, rx, 0);
    {
        const char* gen = ws + WS_GEN;
        asm volatile("" : "+s"(gen));
        tag = *(const __attribute__((address_space(4))) u64*)gen + 0x9E3779B97F4A7C15ull;
    }
    if (w == 6 && tt >= 23) {
        const int rb = (tt - 23) * 8 + hg, g = lane >> 4, pp = lane & 3;
        const rsrc_t rr = mk_rsrc(wo_, WO_BYTES);
        lptr scr = sm + A_SCR + 14336;
        u32x4 v[9];
#pragma unroll
        for (int j = 0; j < 9; ++j) v[j] = ld16<0>(rr, (lane >> 3) * 1152 + (lane & 7) * 16, rb * 9216 + j * 128);
        float s0 = 0.f, s1 = 0.f;
#pragma unroll
        for (int j = 0; j < 9; ++j) {
            *(LAS u32x4*)(scr + lw + j * 128) = v[j];
            s0 = dot2(v[j][0], ONES2, s0); s1 = dot2(v[j][1], ONES2, s1); s0 = dot2(v[j][2], ONES2, s0); s1 = dot2(v[j][3], ONES2, s1);
        }
        float s = s0 + s1;
        s += shfl(s, lane ^ 1); s += shfl(s, lane ^ 2); s += shfl(s, lane ^ 4);
        s *= (1.0f / D_);
        const float hi = bf_lo(pack2(s, 0.f));
        if ((lane & 7) == 0) *(LAS u32x2*)(scr + lw + 1152) = (u32x2){pack2(hi, s - hi), 0u};
        WAIT_L0();
#pragma unroll 1
        for (int k = 0; k < 10; ++k) {
            const int f = 4 * k + g, cb = f / 5, j = f - cb * 5;
            lptr p = scr + lr + ((j == 4 && pp >= 2) ? 1152 : cb * 144 + j * 32 + pp * 8);
            const bf16x8 fr = lds_tr2(p, p + 4 * A_SCR_RS);
            st16(rws, (unsigned)(cb * 92160 + j * 1024 + (lane & 15) * 16), WS_WOF + (rb >> 2) * 5120 + (rb & 3) * 256, __builtin_bit_cast(u32x4, fr));
        }
        WAIT_V(0);
        set_flag(rws, FL_S * 2048 + rb * 8, tag, lane);
    }
    asm volatile("" : "+s"(tag));
    if (w == 7) { a_ones(sm, lane); WAIT_L0(); }
    if (w < 7 && tt < 18) {
        const int g = lane >> 4;
        lptr p0 = sm + A_SCR + w * 2048 + (8 * g + ((lane >> 2) & 3)) * 64 + ((g & 1) * 32) + (lane & 3) * 8;
        lptr p1 = p0 + ((g & 1) ? -32 : 32);
        WAIT_V(4);
        const bf16x8 f0 = lds_tr2(p0, p0 + 256), f1 = lds_tr2(p1, p1 + 256);
        const unsigned o = WS_WQF + ((hg * 18 + tt) * 14 + 2 * w) * 1024;
        st16(rws, lane * 16, o, __builtin_bit_cast(u32x4, f0));
        st16(rws, lane * 16, o + 1024, __builtin_bit_cast(u32x4, f1));
    }
    WAIT_V(0);
    if (tt < 18) {
        BAR();
        if (w == 0) set_flag(rws, FL_Q + hg * 256 + tt * 8, tag, lane);
    }
    a_xdma(c, rx, 1); a_xdma(c, rx, 2);
    WAIT_V0();
    if (w == 7) poll<true>(rws, FL_Q + hg * 256 + (lane < 18 ? lane : 0) * 8, tag);
    else BAR();
    a_we(we, ws, lane, bid, w, w < 7, 0, E_N1);
    if (w == 7) {
#pragma unroll 1
        for (unsigned it = 0; it < (1u << 20); ++it) {
            const u64 f0 = flag_ld(rws, FL_S * 2048 + (64 + (lane & 7)) * 8), f1 = flag_ld(rws, FL_S * 2048 + lane * 8);
            if (__all(f0 == tag && f1 == tag)) break;
        }
    }
}

DI void a_gemm(f32x4 (&acc)[2][5], APre& pre, bf16x8 (&we)[18][2], lptr sm, char* ws, const char* wg_, const char* wu_, const char* wd_,
               const int l_, const int w_, u64 tag) {
    asm volatile("" : "+s"(tag));
    const ACtx c = a_ctx(sm, w_);
    const int lane = c.lane, w = c.w, l = opaque_s(l_), bid = opaque_s(blockIdx.x);
    const srd_t rx = mk_srd(ws + WS_X2, ACT_BYTES), rs = mk_srd(ws, WS_END);
    const rsrc_t rws = mk_rsrc(ws, WS_END);
    const unsigned l16 = lane * 16, wsb = WS_WOF + c.hg * 92160 + w * 1024, wlb = (unsigned)(size_t)sm + w * 1024;
    a_we(we, ws, lane, bid, w, w < 7, E_N1, 18);
    WAIT_V0();
    if (l == 0 && bid >= 128) {
        BAR();
        ffn_warm(sm, wg_, wu_, wd_, lane, bid, w); ffn_warm(sm, wg_, wu_, wd_, lane, bid - 128, w);
        if (w >= 6) ffn_warm(sm, wg_, wu_, wd_, lane, bid - (w & 1) * 128, 8);
#pragma unroll 1
        for (int k = 0; k < SKEW; ++k) __builtin_amdgcn_s_sleep(18);
    }
    if (l) {
        if (w == 7) {
            a_ones(sm, lane);
            int i = 8 * c.tt - 4 + (lane < 12 ? lane : 11); i = i < 0 ? 0 : i;
            WAIT_L0();
            poll<true>(rws, FL_E * 2048 + i * 8, tag);
        } else {
            SCHED();
            BAR();
        }
        a_xdma(c, rx, 0); a_xdma(c, rx, 1);
        WAIT_V(4); BAR();
    }
    bf16x8 tk[2][5];
    unsigned ra[3];
#define B_WD(k) do { if ((k) < 8) dma<SC1>(rs, l16, wsb, (k) * 8192, wlb, B_W1 + (k) * 8192); \
                     else if ((k) < 11) dma<SC1>(rs, l16, wsb, (B_N1 + ((k) - 8) * 8) * 1024, wlb, B_W2 + ((k) - 8) * 8192); \
                     else if (w == 0) { dma<SC1>(rs, l16, wsb, 65536, wlb, B_W1 + 65536); dma<SC1>(rs, l16, wsb, 89 * 1024, wlb, B_W2 + 24576); } } while (0)
#define A_LOOP(MM) \
    a_tok(tk[0], c, 0); \
    _Pragma("unroll") for (int s = 0; s < 18; ++s) { \
        if (s == 5) { if (l) WAIT_V(0); WAIT_L0(); BAR(); }     \
        MM(0, 2); \
        SCHED(); \
        if (s == 0) { if (l) a_xdma(c, rx, 2); } \
        if (s >= 5 && s < 11) { B_WD(2 * (s - 5)); B_WD(2 * (s - 5) + 1); } \
        if (s == 4) a_res(pre, ra, sm, c, 0); \
        if (s == 17) a_res(pre, ra, sm, c, 1); \
        if (s == 16) a_pre(pre, c); \
        MM(2, 5); \
        SCHED(); \
    }
#define A_MS(i0, i1) a_mm<true>(acc, tk[s & 1], ones, ones, s, i0, i1, tk[(s + 1) & 1], c)
#define A_MN(i0, i1) a_mm<false>(acc, tk[s & 1], we[s][0], we[s][1], s, i0, i1, tk[(s + 1) & 1], c)
    if (w == 7) {
        const bf16x8 ones = a_ones_frag();
        __builtin_amdgcn_s_setprio(1);
        A_LOOP(A_MS)
        a_stats_out(acc, sm, lane);
        __builtin_amdgcn_s_setprio(0);
    } else {
        A_LOOP(A_MN)
    }
#undef A_LOOP
#undef B_WD
#undef A_MS
#undef A_MN
}

DI void a_post(f32x4 (&acc)[2][5], const APre& pre, lptr sm, char* ws, const int l_, const int w_, u64 tag) {
    asm volatile("" : "+s"(tag));
    const int lane = opaque_lane();
    const int bid = opaque_s(blockIdx.x), w = opaque_s(w_), l = opaque_s(l_);
    const int tt = a_tt(bid), hg = a_hg(bid);
    const int g = lane >> 4;
    const rsrc_t rws = mk_rsrc(ws, WS_END);
    f32x2 csb[2][2];
#pragma unroll
    for (int j = 0; j < 2; ++j)
#pragma unroll
        for (int r = 0; r < 4; ++r) csb[j][r >> 1][r & 1] = bcast_row0(acc[j][0][r]);
    WAIT_L0();
    SCHED();
    BAR();
    if (w < 7) {
        f32x2 st[5];
#pragma unroll
        for (int i = 0; i < 5; ++i) st[i] = *(const LAS f32x2*)(sm + pre.ost + 128 * i);
        SCHED();
#pragma unroll
        for (int i = 0; i < 5; ++i) {
#pragma unroll
            for (int j = 0; j < 2; ++j) {
                const f32x2 a = (f32x2){acc[j][i][0], acc[j][i][1]} * st[i][1] - csb[j][0] * st[i][0];
                const f32x2 b = (f32x2){acc[j][i][2], acc[j][i][3]} * st[i][1] - csb[j][1] * st[i][0];
                *(LAS u32x2*)(sm + pre.ow + 16 * i * A_RS + j * 32) = (u32x2){pack2(a[0], a[1]), pack2(b[0], b[1])};
            }
        }
    }
    WAIT_V(0);
    WAIT_L0();
    BAR();
    {
        lptr rb = sm + pre.rb;
        u32x4 qf[2]; bf16x8 kf[2][2], vf[3];
#pragma unroll
        for (int hh = 0; hh < 2; ++hh) {
            const u32x2 a = *(const LAS u32x2*)(rb + 16 * A_RS + hh * 8), b = *(const LAS u32x2*)(rb + 16 * A_RS + hh * 8 + 8);
            qf[hh] = (u32x4){a[0], a[1], b[0], b[1]};
#pragma unroll
            for (int k = 0; k < 2; ++k) {
                const u32x2 c = *(const LAS u32x2*)(rb + 144 + 16 * k * A_RS + hh * 8), d = *(const LAS u32x2*)(rb + 144 + 16 * k * A_RS + hh * 8 + 8);
                kf[hh][k] = as_frag((u32x4){c[0], c[1], d[0], d[1]});
            }
        }
        lptr vb = sm + pre.vb;
#pragma unroll
        for (int c = 0; c < 3; ++c) vf[c] = lds_tr2(vb + 32 * c, vb + 32 * c + 16 * A_RS);
        SCHED();
        qf[0][0] = g < 3 ? qf[0][0] : 0u; qf[0][1] = g < 2 ? qf[0][1] : 0u; qf[0][2] = g < 2 ? qf[0][2] : 0u; qf[0][3] = g < 2 ? qf[0][3] : 0u;
        qf[1][0] = g > 1 ? qf[1][0] : 0u; qf[1][1] = g > 1 ? qf[1][1] : 0u; qf[1][2] = g > 1 ? qf[1][2] : 0u; qf[1][3] = g > 0 ? qf[1][3] : 0u;
        f32x4 sc[2][2];
#pragma unroll
        for (int hh = 0; hh < 2; ++hh)
#pragma unroll
            for (int k = 0; k < 2; ++k) sc[hh][k] = mfma16(kf[hh][k], as_frag(qf[hh]), pre.bias[k]);
        constexpr float kScale = 0.23570226039551584f * 1.4426950408889634f;
        float mx[2], sum[2];
#pragma unroll
        for (int hh = 0; hh < 2; ++hh) {
            const float m0 = fmaxf(fmaxf(sc[hh][0][0], sc[hh][0][1]), fmaxf(sc[hh][0][2], sc[hh][0][3]));
            const float m1 = fmaxf(fmaxf(sc[hh][1][0], sc[hh][1][1]), fmaxf(sc[hh][1][2], sc[hh][1][3]));
            mx[hh] = fmaxf(m0, m1);
        }
        mx[0] = red_max_g(mx[0]) * -kScale; mx[1] = red_max_g(mx[1]) * -kScale;
#pragma unroll
        for (int hh = 0; hh < 2; ++hh) {
#pragma unroll
            for (int k = 0; k < 2; ++k)
#pragma unroll
                for (int r = 0; r < 4; ++r) sc[hh][k][r] = __builtin_amdgcn_exp2f(__builtin_fmaf(sc[hh][k][r], kScale, mx[hh]));
            const f32x4 s4 = sc[hh][0] + sc[hh][1];
            sum[hh] = (s4[0] + s4[1]) + (s4[2] + s4[3]);
        }
        sum[0] = __builtin_amdgcn_rcpf(red_sum_g(sum[0])); sum[1] = __builtin_amdgcn_rcpf(red_sum_g(sum[1]));
        f32x4 o[2][2];
#pragma unroll
        for (int hh = 0; hh < 2; ++hh) {
            const f32x4 p0 = sc[hh][0] * sum[hh], p1 = sc[hh][1] * sum[hh];
            const u32x4 pf = {pack2(p0[0], p0[1]), pack2(p0[2], p0[3]), pack2(p1[0], p1[1]), pack2(p1[2], p1[3])};
            o[hh][0] = mfma16(vf[hh], as_frag(pf), (f32x4){0.f, 0.f, 0.f, 0.f});
            o[hh][1] = mfma16(vf[hh + 1], as_frag(pf), (f32x4){0.f, 0.f, 0.f, 0.f});
        }
        const unsigned ob = WS_O + (tt * 4 + (w >> 1)) * 1024;
        st8(rws, pre.oo[0], ob, (u32x2){pack2(o[0][0][0], o[0][0][1]), pack2(o[0][0][2], o[0][0][3])});
        const unsigned e = pack2(o[0][1][0], o[0][1][1]), f = pack2(o[1][0][0], o[1][0][1]);
        st8(rws, pre.oo[1], ob, (u32x2){g == 0 ? e : f, pack2(o[1][0][2], o[1][0][3])});
        if (g == 0) st8(rws, pre.oo[2], ob, (u32x2){pack2(o[1][1][0], o[1][1][1]), pack2(o[1][1][2], o[1][1][3])});
    }
    (void)hg; (void)tag;
}

DI void stage_b(const APre& pre, lptr sm, char* ws, const int l_, const int w_, u64 tag) {
    asm volatile("" : "+s"(tag));
    const int lane = opaque_lane();
    const int bid = opaque_s(blockIdx.x), w = opaque_s(w_), l = opaque_s(l_);
    const int tt = a_tt(bid), cb = a_hg(bid);
    const int t = lane & 15, g = lane >> 4;
    const int ti = w & 3, kh = w >> 2;
    const int token = tt * 64 + ti * 16 + t;
    const rsrc_t rws = mk_rsrc(ws, WS_END);
    const unsigned l16 = lane * 16;

    bf16x8 wf[9][5];
    {
        lptr ba = sm + B_W1 + kh * 46080 + l16, bb = sm + (kh ? B_W2 : B_W1 + 20480) + l16;
#pragma unroll
        for (int s = 0; s < 9; ++s) {
            if (s == 6) {
                SCHED();
                publish(rws, (FL_A + l) * 2048 + (tt * 8 + cb) * 8, tag, w, lane);
            }
#pragma unroll
            for (int j = 0; j < 5; ++j) wf[s][j] = lds_frag(s < 4 ? ba + (s * 5 + j) * 1024 : bb + ((s - 4) * 5 + j) * 1024);
        }
    }
    f32x4 r0[3]; unsigned xo[3];
    float xmean = *(const LAS float*)(sm + A_MEAN + (16 + ti * 16 + t) * 4);
#pragma unroll
    for (int j = 0; j < 3; ++j) {
        r0[j] = (f32x4){bf_lo(pre.res[j][0]), bf_hi(pre.res[j][0]), bf_lo(pre.res[j][1]), bf_hi(pre.res[j][1])} - xmean;
        const int d = cb * 72 + (j < 2 ? 32 * kh + 16 * j : 64) + 4 * g;
        xo[j] = (unsigned)((d >> 5) * 131072 + ((d >> 3) & 3) * 256 + t * 16 + (d & 7) * 2);
        asm volatile("" : "+v"(r0[j]), "+v"(xo[j]));
    }
    unsigned so = (unsigned)(token * 64), bsrc = (unsigned)((32 + t) * 4);
    asm volatile("" : "+v"(so), "+v"(bsrc), "+v"(xmean));
    WAIT_L0();
    SCHED();
    if (bid == 0 && w == 6 && l == 1) {
#pragma unroll 1
        for (unsigned it = 0; it < (1u << 20); ++it) {
            const u64 f0 = flag_ld(rws, FL_A * 2048 + lane * 8), f1 = flag_ld(rws, FL_A * 2048 + 512 + lane * 8);
            const u64 f2 = flag_ld(rws, FL_A * 2048 + 1024 + lane * 8), f3 = flag_ld(rws, FL_A * 2048 + 1536 + lane * 8);
            if (__all(f0 == tag && f1 == tag && f2 == tag && f3 == tag)) break;
        }
        if (lane == 0) st8(rws, 0, WS_GEN, __builtin_bit_cast(u32x2, tag));
    }
    if (w == 7) poll<true>(rws, (FL_A + l) * 2048 + (tt * 8 + (lane & 7)) * 8, tag);
    else BAR();
    bf16x8 of[9];
    const unsigned ob = WS_O + (kh * 9 * 128 + tt * 4 + ti) * 1024;
#pragma unroll
    for (int s = 0; s < 9; ++s) of[s] = as_frag(ld16<SC1>(rws, l16, ob + s * 131072));
    CFENCE(); SCHED();
    f32x4 acc[5];
#pragma unroll
    for (int s = 0; s < 9; ++s) {
#pragma unroll
        for (int j = 0; j < 5; ++j) acc[j] = mfma16(wf[s][j], of[s], s ? acc[j] : (f32x4){0.f, 0.f, 0.f, 0.f});
        SCHED();
    }
    f32x4 own[3];
    {
        lptr rd = sm + B_RED + w * 3072 + l16;
        if (kh) { *(LAS f32x4*)(rd) = acc[0]; *(LAS f32x4*)(rd + 1024) = acc[1]; own[0] = acc[2] + r0[0]; own[1] = acc[3] + r0[1]; }
        else    { *(LAS f32x4*)(rd) = acc[2]; *(LAS f32x4*)(rd + 1024) = acc[3]; own[0] = acc[0] + r0[0]; own[1] = acc[1] + r0[1]; }
        *(LAS f32x4*)(rd + 2048) = acc[4];
        own[2] = acc[4];
    }
    WAIT_L0();
    BAR();
    {
        lptr rd = sm + B_RED + (w ^ 4) * 3072 + l16;
        const f32x4 p2 = *(const LAS f32x4*)(rd + 2048), p0 = *(const LAS f32x4*)(rd), p1 = *(const LAS f32x4*)(rd + 1024);
        own[2] += p2;
        const float dm = __int_as_float(__builtin_amdgcn_ds_bpermute(bsrc, __float_as_int(own[2][0] + own[2][1])));
        own[0] += p0; own[1] += p1; own[2] += r0[2];
        float sq = 0.f;
        const unsigned xb = WS_X1 + (tt * 4 + ti) * 1024;
#pragma unroll
        for (int j = 0; j < 3; ++j) {
            const f32x4 v = own[j] - dm;
            const u32x2 pk = {pack2(v[0], v[1]), pack2(v[2], v[3])};
            if (j < 2 || (g < 2 && kh == 0)) {
                st8(rws, xo[j], xb, pk);
                sq = dot2(pk[0], pk[0], sq); sq = dot2(pk[1], pk[1], sq);
            }
        }
        sq = red_sum_g(sq);
        if (g == 0) st4(rws, so, WS_STATS + cb * 8 + kh * 4, __float_as_uint(sq));
        if (cb == 0 && kh == 0) { if (g == 1) st4(rws, so >> 4, WS_MEAN, __float_as_uint(xmean + dm)); }
    }
    WAIT_L0();
}

constexpr int EB1 = 2;
constexpr int HPRE = 2;
constexpr int C_BUF = 45056;
constexpr int C_S2 = 90112, C_S3 = 126976;
#define C_SLOT(b) (((b) == 0 || ((b) & 3) == 2) ? 0 : ((b) & 3) == 1 ? C_BUF : ((b) & 3) == 3 ? C_S2 : C_S3)

template <int V> struct IC { static constexpr int value = V; };

DI void stage_c(lptr sm, char* ws, const char* wg_, const char* wu_, const char* wd_, const int l_, const int w_, u64 tag) {
    asm volatile("" : "+s"(tag));
    const int lane = opaque_lane();
    const int bid = opaque_s(blockIdx.x), w = opaque_s(w_), l = opaque_s(l_);
    const int fs = 2 * (bid & 7) + ((bid >> 3) & 1), T = bid >> 4;
    const int m = w >> 2, n = w & 3;
    const int g = lane >> 4;
    const int q = (lane >> 2) & 3, pp = lane & 3;
    const rsrc_t rws = mk_rsrc(ws, WS_END);
    const srd_t rx = mk_srd(ws, WS_END), rg = mk_srd(wg_, WFF_BYTES), ru = mk_srd(wu_, WFF_BYTES), rd = mk_srd(wd_, WFF_BYTES);
    const srd_t rs = mk_srd((n & 1) ? wu_ : wg_, WFF_BYTES);
    const unsigned smb = (unsigned)(size_t)sm;

    unsigned so[3];
    const int i0 = (1 - m) * 8 + 2 * n;
#pragma unroll
    for (int k = 0; k < 3; ++k) {
        const int ins = (k < 2) ? i0 + k : 16 + (n >> 1);
        const int qq = ins * 64 + lane;
        const int p = qq / 36, c = qq - p * 36;
        so[k] = (unsigned)(p * 9216 + (c ^ (((p >> 3) & 1) << 1)) * 16 + fs * 576);
    }
    const unsigned lw = smb + i0 * 1024, lx = smb + (n & 1) * 18432 + (16 + (n >> 1)) * 1024, lt = smb + 36864 + w * 1024;
    const unsigned l16 = lane * 16 + WS_X1 + (T * 8 + w) * 1024;
    const unsigned sd = (unsigned)((lane >> 3) * 1152 + ((lane & 7) ^ (((lane >> 4) & 3) << 1)) * 16);
    const int d0 = m ? 4 * n : 16 + 5 * n;
    const unsigned dsb = (unsigned)((fs * 288 + d0 * 8) * 1152), dlb = smb + d0 * 1024;

#define C_W(k, ks, ts, buf) do { \
        if ((k) == 0) dma(rg, so[0], ks, 0, lw + (buf), 0); \
        if ((k) == 1) dma(ru, so[0], ks, 0, lw + (buf), 18432); \
        if ((k) == 2) dma(rg, so[1], ks, 0, lw + (buf), 1024); \
        if ((k) == 3) dma(ru, so[1], ks, 0, lw + (buf), 18432 + 1024); \
        if ((k) == 4) { if (m == 0) dma(rs, so[2], ks, 0, lx + (buf), 0); } \
        if ((k) == 5) dma<SC1>(rx, l16, ts, 0, lt + (buf), 0); \
        } while (0)
#define C_D(k, b, buf) do { \
        if ((k) < 4) dma(rd, sd, dsb, (k) * 9216 + (b) * 128, dlb, (buf) + (k) * 1024); \
        if ((k) == 4) dma_if0(m, rd, sd, dsb, (k) * 9216 + (b) * 128, dlb, (buf) + (k) * 1024); \
        } while (0)

    if (w != 7) {
#pragma unroll
        for (int k = 0; k < 5; ++k) { C_W(k, 0u, 0u, 0); }
        if (m == 0) WAIT_V(5); else WAIT_V(4);
    } else {
        WAIT_V(0);
    }
    BAR();
    if (w == 7) set_flag(rws, (FL_B + l) * 2048 + (a_tt(bid) * 8 + a_hg(bid)) * 8, tag, lane);
    const int rowoff = (8 * g + q) * 576 + pp * 8;
    int be = rowoff + 128 * n + 32 * (g & 1);
    int bo = rowoff + 128 * n - 32 * (g & 1);
    int bm = (8 * g + q) * 576 + (((32 + n) ^ (2 * (g & 1))) * 16) + 8 * (pp & 1) + (pp >> 1) * 18432;
    int bt = 36864 + m * 4096 + lane * 16;
    asm volatile("" : "+v"(be), "+v"(bo), "+v"(bm), "+v"(bt));
    if (w == 7) poll<true>(rws, (FL_B + l) * 2048 + (T * 16 + (lane & 15)) * 8, tag);
    else BAR();
    C_W(5, 0u, 0u, 0);
    if (w == 7) {
#pragma unroll
        for (int k = 0; k < 4; ++k) { C_W(k, 0u, 0u, 0); }
    }
#pragma unroll
    for (int k = 0; k < 5; ++k) { C_W(k, 294912u, 0u, C_BUF); }
    C_W(5, 0u, 131072u, C_BUF);
    f32x4 acc[9][4];
#pragma unroll
    for (int j = 0; j < 9; ++j)
#pragma unroll
        for (int i = 0; i < 4; ++i) {
            f32x2 z0, z1;
            asm volatile("v_mov_b64 %0, 0\n\tv_mov_b64 %1, 0" : "=v"(z0), "=v"(z1));
            acc[j][i] = __builtin_shufflevector(z0, z1, 0, 1, 2, 3);
        }

    lptr pe = sm + be, po1 = sm + bo, pm = sm + bm, pt = sm + bt;
    unsigned qw = lw + 2 * C_BUF, qx = lx + 2 * C_BUF, qt = lt + 2 * C_BUF;
    unsigned ks = 2 * 294912u, ts = 2 * 131072u;
    int c3 = 0, dr, dw;
#define C_FILLW(k) do { \
        if ((k) == 0) dma(rg, so[0], ks, 0, qw, 0); \
        if ((k) == 1) dma(ru, so[0], ks, 0, qw, 18432); \
        if ((k) == 2) dma(rg, so[1], ks, 0, qw, 1024); \
        if ((k) == 3) dma(ru, so[1], ks, 0, qw, 18432 + 1024); \
        if ((k) == 4) dma_if0(m, rs, so[2], ks, 0, qx, 0); \
        if ((k) == 5) dma<SC1>(rx, l16, ts, 0, qt, 0); \
        } while (0)
#define C_FILLD0(k) C_D(k, 0, 0)
#define C_FILLD1(k) C_D(k, 1, C_BUF)
#define C_RW(j) do { lptr p = (((j) & 1) ? po1 : pe) + 32 * (j); wf[j] = lds_tr2(p, p + 2304); wf[4 + (j)] = lds_tr2(p + 18432, p + 18432 + 2304); CFENCE(); } while (0)
#define C_ADV(z) do { \
        if ((z) == 2) { dr = (c3 == 2) ? -2 * C_BUF : C_BUF; asm volatile("" : "+s"(dr)); } \
        if ((z) == 4) { dw = (c3 == 0) ? -2 * C_BUF : C_BUF; asm volatile("" : "+s"(dw)); } \
        if ((z) == 6) { c3 = (c3 == 2) ? 0 : c3 + 1; asm volatile("" : "+s"(c3)); } \
        if ((z) == 9) { pe += dr; asm volatile("" : "+v"(pe)); } \
        if ((z) == 11) { po1 += dr; asm volatile("" : "+v"(po1)); } \
        if ((z) == 13) { pm += dr; asm volatile("" : "+v"(pm)); } \
        if ((z) == 15) { pt += dr; asm volatile("" : "+v"(pt)); } \
        if ((z) == 17) { qw += dw; asm volatile("" : "+s"(qw)); } \
        if ((z) == 19) { qx += dw; asm volatile("" : "+s"(qx)); } \
        if ((z) == 21) { qt += dw; asm volatile("" : "+s"(qt)); } \
        if ((z) == 23) { ks += 294912u; asm volatile("" : "+s"(ks)); } \
        if ((z) == 25) { ts += 131072u; asm volatile("" : "+s"(ts)); } \
        SCHED(); } while (0)
#define C_STEP1(ST, NX, NY, LAST) do { \
        bf16x8 tk[4], wf[9]; \
        ST(0); \
        _Pragma("unroll") for (int i = 0; i < 4; ++i) tk[i] = lds_frag(pt + i * 1024); \
        CFENCE(); \
        ST(1); C_RW(0); ST(2); C_RW(1); ST(3); C_RW(2); ST(4); C_RW(3); ST(5); \
        wf[8] = lds_tr2(pm, pm + 2304); \
        asm volatile("s_cmp_eq_u32 %0, 0\n\ts_cbranch_scc1 .Lw%=\n\ts_waitcnt vmcnt(" #NY ")\n.Lw%=:" :: "s"(m) : "memory", "scc");     \
        WAIT_L0(); SCHED(); BAR(); \
        __builtin_amdgcn_s_setprio(1); \
        _Pragma("unroll") for (int z = 0; z < 36; ++z) { \
            if (z == 36 - EB1) { SCHED(); __builtin_amdgcn_s_setprio(3); WAIT_V(NX); if (!(LAST) || m == 0) BAR(); SCHED(); }     \
            const int i_ = (z & 4) ? 3 - (z & 3) : z & 3;     \
            mfma_acc(acc[z >> 2][i_], wf[z >> 2], tk[i_]); \
            C_ADV(z); } \
        __builtin_amdgcn_s_setprio(0); \
        SCHED(); \
    } while (0)

    WAIT_V(5);
    BAR();
    if (m == 1) BAR();
#pragma unroll 1
    while (ts != 18 * 131072u) C_STEP1(C_FILLW, 6, 5, 0);
    C_STEP1(C_FILLD0, 5, 4, 0);
    C_STEP1(C_FILLD1, 5, 4, 1);
    lptr hb = sm + (m ? C_S3 : C_S2);
    const int lane2 = opaque_lane();
    const int t2 = lane2 & 15, g2 = lane2 >> 4;
    {
        lptr hm = hb + ((g2 >> 1) * 9 + 8) * 1024 + (2 * (n & 1) + (g2 & 1)) * 256 + t2 * 16 + (n >> 1) * 8;
        lptr hw = hb + 2 * n * 1024 + lane2 * 16;
        asm volatile("s_nop 7" : "+v"(hm), "+v"(hw));
        if (m == 1) __builtin_amdgcn_s_setprio(2);
#pragma unroll
        for (int k = 0; k < 8; ++k) {
            const int j = 2 * (k >> 2), i = k & 3;
            f32x4 h0, h1;
#pragma unroll
            for (int r = 0; r < 4; ++r) { h0[r] = relu(acc[j][i][r]); h1[r] = relu(acc[j + 1][i][r]); }
            SCHED();
            h0 *= acc[4 + j][i]; h1 *= acc[5 + j][i];
            SCHED();
            *(LAS u32x4*)(hw + (i * 9 + (k >> 2)) * 1024) = (u32x4){pack2(h0[0], h0[1]), pack2(h0[2], h0[3]), pack2(h1[0], h1[1]), pack2(h1[2], h1[3])};
            SCHED();
        }
        f32x4 gq[2], uq[2];
#pragma unroll
        for (int i = 0; i < 2; ++i)
#pragma unroll
            for (int r = 0; r < 4; ++r) {
                auto x = __builtin_amdgcn_permlane32_swap(__float_as_uint(acc[8][2 * i][r]), __float_as_uint(acc[8][2 * i + 1][r]), false, false);
                gq[i][r] = relu(__uint_as_float(x[0])); uq[i][r] = __uint_as_float(x[1]);
            }
        gq[0] *= uq[0]; gq[1] *= uq[1];
        *(LAS u32x2*)(hm) = (u32x2){pack2(gq[0][0], gq[0][1]), pack2(gq[0][2], gq[0][3])};
        *(LAS u32x2*)(hm + 18432) = (u32x2){pack2(gq[1][0], gq[1][1]), pack2(gq[1][2], gq[1][3])};
        __builtin_amdgcn_s_setprio(0);
        WAIT_V(0);
        WAIT_L0(); SCHED(); BAR();
    }
    bf16x8 hf[4][9];
    const int bd = (lane2 >> 2) * 128 + ((n ^ ((lane2 >> 3) & 3)) * 32) + (lane2 & 3) * 8;
    const int sw = (fs >> 2) & 1;
    const u64 ownm = (m == (fs >> 3)) ? 0x000f000f000f000full << (4 * (fs & 3)) : 0ull;
    unsigned po[2];
    po[1] = (unsigned)((t2 >> 2) * 9216 + g2 * 64 + (t2 & 3) * 16);
    po[0] = ((ownm >> lane2) & 1) ? 0x7ffffff0u : po[1];
    unsigned pl = smb + 36864 + n * 256 + g2 * 64 + (t2 & 3) * 16;
    asm volatile("" : "+v"(po[0]), "+v"(po[1]), "+v"(pl));
    const unsigned pb0 = WS_PART + fs * ACT_BYTES + (T * 2 + m) * 73728 + n * 256 + sw * 36864, pb1 = pb0 + 36864 - sw * 73728;
    lptr pws[4] = {sm + bd, sm + C_BUF + bd, sm + C_S2 + bd, sm + C_S3 + bd};
    lptr hr[2] = {hb + lane2 * 16 + sw * 18432, hb + lane2 * 16 + 18432 - sw * 18432};
    asm volatile("" : "+v"(pws[0]), "+v"(pws[1]), "+v"(pws[2]), "+v"(pws[3]), "+v"(hr[0]), "+v"(hr[1]));
    f32x4 a2[4];
    u32x4 pk[2];
#define C_MEND(b) do { if (b >= 2 && b <= 6) WAIT_V(9); else if (b == 7) WAIT_V(4);     \
        if (b < 8 || m == 0) BAR(); } while (0)
#define C_PACK(b, nop) do { \
        asm volatile(nop : "+v"(a2[0]), "+v"(a2[1]), "+v"(a2[2]), "+v"(a2[3]));     \
        _Pragma("unroll") for (int e = 0; e < 8; ++e) pk[e >> 2][e & 3] = pack2(a2[e >> 1][2 * (e & 1)], a2[e >> 1][2 * (e & 1) + 1]); \
        asm volatile("s_mov_b64 exec, %2\n\tds_write_b128 %0, %1 offset:%3\n\ts_mov_b64 exec, -1" \
                     :: "v"(pl), "v"(pk[0]), "s"(ownm), "i"((b) < 8 ? (b) * 1024 : 45056) : "memory"); \
        } while (0)
#define C_STORE(b) do { st16(rws, po[0], pb0 + (b) * 1024, pk[0]); st16(rws, po[1], pb1 + (b) * 1024, pk[1]); } while (0)
    auto block = [&](auto bc) __attribute__((always_inline)) {
        constexpr int b = decltype(bc)::value;
        bf16x8 wf[9];
        lptr pw = pws[C_SLOT(b) == 0 ? 0 : C_SLOT(b) == C_BUF ? 1 : C_SLOT(b) == C_S2 ? 2 : 3];
        if constexpr (b == 0) {
#pragma unroll
            for (int s = 0; s < HPRE; ++s)
#pragma unroll
                for (int i = 0; i < 4; ++i) hf[i][s] = lds_frag(hr[i >> 1] + ((i & 1) * 9 + s) * 1024);
#pragma unroll
            for (int s = 0; s < 9; ++s) wf[s] = lds_tr2(pw + s * 4096, pw + s * 4096 + 2048);
        } else {
#pragma unroll
            for (int s = 0; s < 9; ++s) {
                if (b + 2 < 9 && (s == 0 || (s & 1))) C_D((s + 1) >> 1, b + 2, C_SLOT(b + 2));
                wf[s] = lds_tr2(pw + s * 4096, pw + s * 4096 + 2048);
                CFENCE();
                if (s == 6) C_PACK(b - 1, "");
            }
            C_STORE(b - 1);
        }
        if (m == 1) {
            if (b == 1) WAIT_V(6); else if (b >= 2 && b <= 6) WAIT_V(8); else if (b == 7) WAIT_V(4);
        }
        if (b == 0) { if (m == 1) {
            WAIT_L0(); BAR();
            const unsigned s2 = (unsigned)((fs * 288 + n * 72) * 1152), l2 = smb + n * 9216;
#pragma unroll
            for (int k = 0; k < 9; ++k) dma(rd, sd, s2, k * 9216 + 256, l2, C_SLOT(2) + k * 1024);
        } }
        WAIT_L0();
        SCHED();
        BAR();
        __builtin_amdgcn_s_setprio(1);
#pragma unroll
        for (int s = 0; s < 9; ++s) {
#pragma unroll
            for (int i = 0; i < 4; ++i) {
                if (b > 0 && s * 4 + i == 36 - EB1) { SCHED(); __builtin_amdgcn_s_setprio(3); C_MEND(b); SCHED(); }
                if (b == 0 && s + HPRE < 9) { hf[i][s + HPRE] = lds_frag(hr[i >> 1] + ((i & 1) * 9 + s + HPRE) * 1024); SCHED(); }
                if (s == 0) mfma_zero(a2[i], wf[s], hf[i][s]); else mfma_acc(a2[i], wf[s], hf[i][s]);
                if (b == 0) SCHED();
            }
        }
        __builtin_amdgcn_s_setprio(0);
        SCHED();
        if (b == 0) C_MEND(b);
    };
    block(IC<0>{}); block(IC<1>{}); block(IC<2>{}); block(IC<3>{}); block(IC<4>{}); block(IC<5>{}); block(IC<6>{}); block(IC<7>{}); block(IC<8>{});
    C_PACK(8, "s_nop 13");
    C_STORE(8);
    asm volatile("s_waitcnt lgkmcnt(0)" ::: "memory");
#undef C_W
#undef C_D
#undef C_FILLW
#undef C_FILLD0
#undef C_FILLD1
#undef C_RW
#undef C_ADV
#undef C_STEP1
#undef C_MEND
#undef C_PACK
#undef C_STORE
}

DI bool stage_e(bf16x8 (&we)[18][2], lptr sm, char* ws, char* xout, const int l_, const int w_, u64 tag) {
    asm volatile("" : "+s"(tag));
    const int lane = opaque_lane();
    const int bid = opaque_s(blockIdx.x), w = opaque_s(w_), l = opaque_s(l_);
    const int fs = 2 * (bid & 7) + ((bid >> 3) & 1), T = bid >> 4;
    const int R = T * 16 + fs;
    const int t = lane & 15, g = lane >> 4;
    const rsrc_t rws = mk_rsrc(ws, WS_END), ro = mk_rsrc(xout, ACT_BYTES);
    const srd_t rx = mk_srd(ws, WS_END);

    const int tok = (R >> 2) * 32 + (R & 3) * 4 + (t & 3) + (g & 1) * 16;
    const u32x2 sv = ld8<SC1>(rws, (unsigned)(tok * 64 + ((t >> 2) + 4 * (g >> 1)) * 8), WS_STATS);
    const unsigned mean = ld4<SC1>(rws, (unsigned)(tok * 4), WS_MEAN);
    unsigned xo[3]; u32x2 res[3];
#pragma unroll
    for (int k = 0; k < 3; ++k) {
        const int bn = k < 2 ? 16 * k + 2 * w + (g >> 1) : 32 + (w & 3);
        const int d = 16 * bn + 4 * (t >> 2);
        xo[k] = (unsigned)(tok * 1152 + d * 2);
        res[k] = ld8<SC1>(rws, (unsigned)((d >> 5) * 131072 + (tok >> 4) * 1024 + ((d >> 3) & 3) * 256 + (tok & 15) * 16 + (d & 7) * 2), WS_X1);
        asm volatile("" : "+v"(xo[k]));
    }
    const unsigned fpeer = (FL_C + l) * 2048 + (T * 16 + 2 * w + (lane & 1)) * 8;
    u64 fv = flag_ld(rws, fpeer);
    WAIT_V(6);
    BAR();
    if (w == 7) set_flag(rws, (FL_C + l) * 2048 + R * 8, tag, lane);
    const bool mine = (2 * w + (lane & 1)) == fs;
    const bool ready = __all(fv == tag || mine);
    asm volatile("" :: "v"(sv), "v"(mean), "v"(res[0]), "v"(res[1]), "v"(res[2]));
    const unsigned esb = WS_PART + 2 * w * ACT_BYTES + R * 9216, elb = (unsigned)(size_t)sm + w * 18432 + (w < 2 ? 0 : w < 4 ? 8192 : 16384);
#define E_FETCH() _Pragma("unroll") for (int p = 0; p < 2; ++p) { if (2 * w + p != fs) { \
        _Pragma("unroll") for (int k = 0; k < 9; ++k) dma<SC1>(rx, lane * 16, esb, p * (int)ACT_BYTES + k * 1024, elb, (p * 9 + k) * 1024); } }
    if (ready) { E_FETCH(); }
    a_we(we, ws, lane, bid, w, w < 7 && l == 0, 0, E_N1);
    if (!ready) {
#pragma unroll 1
        for (unsigned it = 0; it < (1u << 20); ++it) {
            if (__all(flag_ld(rws, fpeer) == tag || mine)) break;
            __builtin_amdgcn_s_sleep(1);
        }
        E_FETCH();
    }
#undef E_FETCH
    float sq = __uint_as_float(sv[0]) + __uint_as_float(sv[1]);
    sq += shfl(sq, lane ^ 4); sq += shfl(sq, lane ^ 8); sq += shfl(sq, lane ^ 32);
    float rsq = __builtin_amdgcn_rcpf(sq * (1.0f / D_) + LN_EPS);
    f32x4 r0[3];
#pragma unroll
    for (int k = 0; k < 3; ++k) {
        r0[k] = (f32x4){bf_lo(res[k][0]), bf_hi(res[k][0]), bf_lo(res[k][1]), bf_hi(res[k][1])} + __uint_as_float(mean);
        asm volatile("" : "+v"(r0[k]));
    }
    asm volatile("" : "+v"(rsq));
    u32x4 sel[2];
#pragma unroll
    for (int e = 0; e < 4; ++e) {
        sel[0][e] = (t == 2 * e) ? 0x00003f80u : ((t == 2 * e + 1) ? 0x3f800000u : 0u);
        sel[1][e] = (t == 8 + 2 * e) ? 0x00003f80u : ((t == 9 + 2 * e) ? 0x3f800000u : 0u);
    }
    asm volatile("" : "+v"(sel[0]), "+v"(sel[1]));
    lptr eb[4], e4[4];
#pragma unroll
    for (int f = 0; f < 4; ++f) {
        const bool o = 4 * f + g == fs;
        const int base = (f == 0 ? 0 : f == 1 ? C_BUF : f == 2 ? C_S2 : C_S3) + g * 9216;
        eb[f] = sm + (o ? 36864 : base) + t * 16 + w * 512;
        e4[f] = sm + (o ? 81920 : base + 8192) + t * 16 + (w & 3) * 256;
        asm volatile("" : "+v"(eb[f]), "+v"(e4[f]));
    }
    WAIT_V(0);
    WAIT_L0();
    BAR();
    {
        f32x4 a[3];
#pragma unroll
        for (int h = 0; h < 2; ++h) {
            bf16x8 v[2][5];
#pragma unroll
            for (int f = 0; f < 2; ++f) {
                lptr p = eb[2 * h + f];
                v[f][0] = lds_frag(p); v[f][1] = lds_frag(p + 4096); v[f][2] = lds_frag(p + 256); v[f][3] = lds_frag(p + 4096 + 256);
                v[f][4] = lds_frag(e4[2 * h + f]);
            }
            SCHED();
#pragma unroll
            for (int f = 0; f < 2; ++f) {
                const f32x4 z = {0.f, 0.f, 0.f, 0.f};
                a[0] = mfma16(as_frag(sel[0]), v[f][0], (h | f) ? a[0] : z);
                a[1] = mfma16(as_frag(sel[0]), v[f][1], (h | f) ? a[1] : z);
                a[2] = mfma16(as_frag(sel[0]), v[f][4], (h | f) ? a[2] : z);
                a[0] = mfma16(as_frag(sel[1]), v[f][2], a[0]);
                a[1] = mfma16(as_frag(sel[1]), v[f][3], a[1]);
            }
        }
#pragma unroll
        for (int k = 0; k < 3; ++k) {
            const f32x4 o = a[k] * rsq + r0[k];
            if (k < 2 || (g < 2 && w < 4)) st8(ro, xo[k], 0, (u32x2){pack2(o[0], o[1]), pack2(o[2], o[3])});
        }
    }
    if (l) {
        return true;
    }
    publish(rws, FL_E * 2048 + R * 8, tag, w, lane);
    return false;
}

__global__ __launch_bounds__(512, 2) void fused_layers(const char*, const char*, const char*, const char*, const char*, const char*, char*, char*) {
    typedef u64 u64x8 __attribute__((ext_vector_type(8)));
    u64x8 ka;
    asm volatile("s_load_dwordx16 %0, %1, 0x0" : "=s"(ka) : "s"(__builtin_amdgcn_kernarg_segment_ptr()));
    extern __shared__ __attribute__((aligned(16))) char smem_[];
    lptr sm = (lptr)smem_;
    const int w = __builtin_amdgcn_readfirstlane(threadIdx.x >> 6);
    if (w == 7) {
        u64 pc; unsigned len;
        asm volatile("s_getpc_b64 %0\n.Lfl_code_begin:\n\ts_mov_b32 %1, .Lfl_code_end-.Lfl_code_begin" : "=s"(pc), "=s"(len));
        const unsigned o = opaque_lane() * 128 + ((blockIdx.x >> 3) & 7) * 8192;
        if ((int)len > 64) warm(mk_srd((const void*)pc, len), o < len - 4 ? o : 0u, 0u, (unsigned)(size_t)sm + A_SCRAP);
    }
    asm volatile("s_waitcnt lgkmcnt(0)" : "+s"(ka));
    const char *x0 = (const char*)ka[0], *wqkv = (const char*)ka[1], *wo = (const char*)ka[2], *wg = (const char*)ka[3], *wu = (const char*)ka[4], *wd = (const char*)ka[5];
    char *out = (char*)ka[6], *ws = (char*)ka[7];
    u64 tag;
    bf16x8 we[18][2];
    stage_0(we, sm, ws, x0, wqkv, wo, w, tag);
#pragma unroll 1
    for (int l = 0;; ++l) {
        f32x4 acc[2][5];
        APre pre;
        a_gemm(acc, pre, we, sm, ws, wg, wu, wd, l, w, tag);
        a_post(acc, pre, sm, ws, l, w, tag);
        stage_b(pre, sm, ws, l, w, tag);
        stage_c(sm, ws, wg, wu, wd, l, w, tag);
        if (stage_e(we, sm, ws, l == 0 ? ws + WS_X2 : out, l, w, tag)) break;
    }
    asm volatile(".Lfl_code_end:");
}

extern "C" void kernel_launch(void* const* d_in, const int* in_sizes, int n_in, void* d_out, int out_size, void* d_ws, size_t ws_size,
                              hipStream_t stream) {
    (void)in_sizes; (void)n_in; (void)out_size; (void)ws_size;
    (void)hipFuncSetAttribute((const void*)fused_layers, hipFuncAttributeMaxDynamicSharedMemorySize, 163840);
    fused_layers<<<256, 512, 163840, stream>>>((const char*)d_in[0], (const char*)d_in[1], (const char*)d_in[2], (const char*)d_in[4],
                                               (const char*)d_in[5], (const char*)d_in[6], (char*)d_out, (char*)d_ws);
}
```
